```python
import torch

def _softmax(x, axis=-1):
    return torch.softmax(x, dim=axis)
def _gelu(x):
    return torch.nn.functional.gelu(x, approximate="tanh")
def _silu(x):
    return torch.nn.functional.silu(x)
def _sigmoid(x):
    return torch.sigmoid(x)
def _layernorm(x, axis=-1, eps=1e-5):
    m = x.mean(dim=axis, keepdim=True)
    v = x.var(dim=axis, keepdim=True, unbiased=False)
    return (x - m) / torch.sqrt(v + eps)
def _scatter_add(tgt, idx, src, axis=0):
    return tgt.clone().index_add_(axis, idx, src)
def _segment_sum(src, seg, n):
    out = torch.zeros((n,) + src.shape[1:], dtype=src.dtype)
    return out.index_add_(0, seg, src)
def _pad(x, pw):
    flat = tuple(v for pair in reversed(pw) for v in pair)
    return torch.nn.functional.pad(x, flat)

kernel_name = 'gated_mix__ssm_conv_s5__swiglu_ffn__prompt'

def setup_inputs():

    _w = (lambda a: (lambda t: t.to(torch.bfloat16) if t.is_floating_point() else t)(torch.as_tensor(a)))
    def _A_setup():
        B, S, D = 1, 2048, 640
        P = D
        Lambda_re = -0.5 * (lambda _x: torch.exp(torch.as_tensor(_x)))((lambda *s: torch.randn(*s, dtype=torch.bfloat16) * 0.2)(P))
        Lambda_im = (lambda *s: torch.randn(*s, dtype=torch.bfloat16) * 0.4)(P) * 1.0
        B_re = (lambda *s: torch.randn(*s, dtype=torch.bfloat16) * 0.2)(P, D) * (D ** -0.5)
        B_im = (lambda *s: torch.randn(*s, dtype=torch.bfloat16) * 0.2)(P, D) * (D ** -0.5)
        C_re = (lambda *s: torch.randn(*s, dtype=torch.bfloat16) * 0.2)(D, P) * (P ** -0.5)
        C_im = (lambda *s: torch.randn(*s, dtype=torch.bfloat16) * 0.2)(D, P) * (P ** -0.5)
        log_dt = (lambda *s: torch.randn(*s, dtype=torch.bfloat16) * 0.2)(P) * 0.3 - 2.0
        Dskip = (lambda *s: torch.randn(*s, dtype=torch.bfloat16) * 0.05)(D)
        return {
            "x": _w((lambda *s: torch.randn(*s, dtype=torch.bfloat16) * 0.05)(B, S, D)),
            "Lambda_re": _w(Lambda_re),
            "Lambda_im": _w(Lambda_im),
            "B_re": _w(B_re),
            "B_im": _w(B_im),
            "C_re": _w(C_re),
            "C_im": _w(C_im),
            "log_dt": _w(log_dt),
            "Dskip": _w(Dskip),
        }
    _A_in = _A_setup()

    def _B_setup():
        B, S, D, F = 1, 2048, 640, 1728
        return {
            "x": _w((lambda *s: torch.randn(*s, dtype=torch.bfloat16) * 0.05)(B, S, D)),
            "w_gate": _w((lambda *s: torch.randn(*s, dtype=torch.bfloat16) * 0.05)(D, F)),
            "w_up": _w((lambda *s: torch.randn(*s, dtype=torch.bfloat16) * 0.05)(D, F)),
            "w_down": _w((lambda *s: torch.randn(*s, dtype=torch.bfloat16) * 0.05)(F, D)),
        }
    _B_in = _B_setup()

    return {
        "A_x": _A_in["x"],
        "A_Lambda_re": _A_in["Lambda_re"],
        "A_Lambda_im": _A_in["Lambda_im"],
        "A_B_re": _A_in["B_re"],
        "A_B_im": _A_in["B_im"],
        "A_C_re": _A_in["C_re"],
        "A_C_im": _A_in["C_im"],
        "A_log_dt": _A_in["log_dt"],
        "A_Dskip": _A_in["Dskip"],
        "B_x": _B_in["x"],
        "B_w_gate": _B_in["w_gate"],
        "B_w_up": _B_in["w_up"],
        "B_w_down": _B_in["w_down"],
        "gate_w": _w((lambda *s: torch.randn(*s, dtype=torch.bfloat16) * 0.05)(640) * 16.0),
    }

def reference(A_x, A_Lambda_re, A_Lambda_im, A_B_re, A_B_im, A_C_re, A_C_im, A_log_dt, A_Dskip, B_x, B_w_gate, B_w_up, B_w_down, gate_w):
    def _A_ref(x, Lambda_re, Lambda_im, B_re, B_im, C_re, C_im, log_dt, Dskip):
        Bsz, L, H = x.shape
        Lam = Lambda_re + 1j * Lambda_im
        Bmat = B_re + 1j * B_im
        Cmat = C_re + 1j * C_im
        dt = torch.exp(log_dt)
        Lam_bar = torch.exp(dt * Lam)
        B_bar = ((Lam_bar - 1.0) / Lam)[:, None] * Bmat
        Bu = torch.einsum("bld,pd->blp", x + 0j, B_bar)
        h = torch.zeros_like(Bu[:, 0, :])
        hs = []
        for t in range(L):
            h = Lam_bar[None, :] * h + Bu[:, t, :]
            hs.append(h)
        hseq = torch.stack(hs, 1)
        y = 2.0 * torch.einsum("blp,dp->bld", hseq, Cmat).real + x * Dskip[None, None, :]
        return y

    def _B_ref(x, w_gate, w_up, w_down):
        g = _silu(x @ w_gate)
        u = x @ w_up
        return (g * u) @ w_down

    x = A_x
    g = _sigmoid((x * gate_w).sum(-1, keepdims=True))
    a = _A_ref(x, A_Lambda_re, A_Lambda_im, A_B_re, A_B_im, A_C_re, A_C_im, A_log_dt, A_Dskip)
    b = _B_ref(x, B_w_gate, B_w_up, B_w_down)
    return g * a + (1 - g) * b
```

```cpp
#include <hip/hip_runtime.h>
#include <hip/hip_bf16.h>
typedef unsigned short u16;
__device__ __forceinline__ float bf2f(u16 v){ return __uint_as_float(((unsigned)v)<<16); }
__device__ __forceinline__ u16 f2bf(float f){ unsigned u=__float_as_uint(f); u += 0x7fffu + ((u>>16)&1u); return (u16)(u>>16); }
__device__ __forceinline__ float rbf(float f){ return bf2f(f2bf(f)); }
constexpr int L=2048,D=640,P=640,F=1728;

__global__ void k_prep(const u16* Lre,const u16* Lim,const u16* logdt,float* lam  ){
  int p=blockIdx.x*blockDim.x+threadIdx.x; if(p>=P) return;
  float dt=rbf(expf(bf2f(logdt[p])));
  float lr=bf2f(Lre[p]), li=bf2f(Lim[p]);
  float ar=dt*lr, ai=dt*li;
  float e=expf(ar); float s,c; sincosf(ai,&s,&c);
  float br=e*c, bi=e*s;
  float nr=br-1.0f, ni=bi;
  float den=lr*lr+li*li;
  float cr=(nr*lr+ni*li)/den, ci=(ni*lr-nr*li)/den;
  lam[4*p]=br; lam[4*p+1]=bi; lam[4*p+2]=cr; lam[4*p+3]=ci;
}
__global__ void k_gate(const u16* x,const u16* gw,float* g){
  int t=blockIdx.x; int lane=threadIdx.x; float s=0;
  for(int d=lane;d<D;d+=64) s+=rbf(bf2f(x[t*D+d])*bf2f(gw[d]));
  for(int m=32;m>=1;m>>=1) s+=__shfl_xor(s,m);
  if(lane==0){ float sb=rbf(s); g[t]=rbf(1.0f/(1.0f+expf(-sb))); }
}
__global__ void k_bu(const u16* x,const u16* Bre,const u16* Bim,const float* lam,float* bu){
  int p=blockIdx.x*blockDim.x+threadIdx.x; int t=blockIdx.y; if(p>=P) return;
  float sr=0,si=0;
  for(int d=0;d<D;++d){ float xv=bf2f(x[t*D+d]); sr+=xv*bf2f(Bre[p*D+d]); si+=xv*bf2f(Bim[p*D+d]); }
  float cr=lam[4*p+2],ci=lam[4*p+3];
  bu[(t*P+p)*2]=cr*sr-ci*si; bu[(t*P+p)*2+1]=cr*si+ci*sr;
}
__global__ void k_scan(const float* lam,float* bu){
  int p=blockIdx.x*blockDim.x+threadIdx.x; if(p>=P) return;
  float lr=lam[4*p],li=lam[4*p+1]; float hr=0,hi=0;
  for(int t=0;t<L;++t){ float br=bu[(t*P+p)*2],bi=bu[(t*P+p)*2+1]; float nr=lr*hr-li*hi+br, ni=lr*hi+li*hr+bi; hr=nr;hi=ni; bu[(t*P+p)*2]=hr; bu[(t*P+p)*2+1]=hi; }
}
__global__ void k_ffn1(const u16* x,const u16* wg,const u16* wu,u16* act){
  int f=blockIdx.x*blockDim.x+threadIdx.x; int t=blockIdx.y; if(f>=F) return;
  float sg=0,su=0;
  for(int d=0;d<D;++d){ float xv=bf2f(x[t*D+d]); sg+=xv*bf2f(wg[d*F+f]); su+=xv*bf2f(wu[d*F+f]); }
  float g=rbf(sg); g=rbf(g/(1.0f+expf(-g))); float u=rbf(su);
  act[t*F+f]=f2bf(g*u);
}
__global__ void k_out(const u16* x,const float* h,const u16* Cre,const u16* Cim,const u16* Dsk,const u16* act,const u16* wd,const float* g,float* out){
  int d=blockIdx.x*blockDim.x+threadIdx.x; int t=blockIdx.y; if(d>=D) return;
  float y=0; for(int p=0;p<P;++p){ y+=h[(t*P+p)*2]*bf2f(Cre[d*P+p])-h[(t*P+p)*2+1]*bf2f(Cim[d*P+p]); }
  float a=2.0f*y+rbf(bf2f(x[t*D+d])*bf2f(Dsk[d]));
  float b=0; for(int f=0;f<F;++f) b+=bf2f(act[t*F+f])*bf2f(wd[f*D+d]);
  b=rbf(b); float gg=g[t]; float om=rbf(1.0f-gg);
  out[t*D+d]=gg*a+rbf(om*b);
}
extern "C" void kernel_launch(void* const* d_in,const int* in_sizes,int n_in,void* d_out,int out_size,void* d_ws,size_t ws_size,hipStream_t stream){
  const u16* x=(const u16*)d_in[0]; const u16* Lre=(const u16*)d_in[1]; const u16* Lim=(const u16*)d_in[2];
  const u16* Bre=(const u16*)d_in[3]; const u16* Bim=(const u16*)d_in[4]; const u16* Cre=(const u16*)d_in[5]; const u16* Cim=(const u16*)d_in[6];
  const u16* logdt=(const u16*)d_in[7]; const u16* Dsk=(const u16*)d_in[8];
  const u16* wg=(const u16*)d_in[10]; const u16* wu=(const u16*)d_in[11]; const u16* wd=(const u16*)d_in[12]; const u16* gw=(const u16*)d_in[13];
  char* ws=(char*)d_ws; float* lam=(float*)ws; float* g=(float*)(ws+16384); float* bu=(float*)(ws+65536); u16* act=(u16*)(ws+65536+(size_t)L*P*8);
  k_prep<<<(P+63)/64,64,0,stream>>>(Lre,Lim,logdt,lam);
  k_gate<<<L,64,0,stream>>>(x,gw,g);
  k_bu<<<dim3((P+127)/128,L),128,0,stream>>>(x,Bre,Bim,lam,bu);
  k_scan<<<(P+63)/64,64,0,stream>>>(lam,bu);
  k_ffn1<<<dim3((F+127)/128,L),128,0,stream>>>(x,wg,wu,act);
  k_out<<<dim3((D+127)/128,L),128,0,stream>>>(x,bu,Cre,Cim,Dsk,act,wd,g,(float*)d_out);
}
```

```cpp
#include <hip/hip_runtime.h>
#include <stdint.h>

namespace {

typedef unsigned short u16;
typedef __bf16 bf16x8 __attribute__((ext_vector_type(8)));
typedef __bf16 bf16x2 __attribute__((ext_vector_type(2)));
typedef short s16x4 __attribute__((ext_vector_type(4)));
typedef short s16x8 __attribute__((ext_vector_type(8)));
typedef float f32x2 __attribute__((ext_vector_type(2)));
typedef float f32x4 __attribute__((ext_vector_type(4)));
typedef unsigned u32x2 __attribute__((ext_vector_type(2)));
typedef unsigned u32x4 __attribute__((ext_vector_type(4)));
#define LAS __attribute__((address_space(3)))
#define GAS __attribute__((address_space(1)))
#define DI __device__ __forceinline__

constexpr int L = 2048, D = 640, F = 1728;
constexpr int PM = 160, NPAN = 13;
constexpr int KS2 = 94, KSF = 54;
constexpr unsigned WS_EPOCH = 0;
constexpr unsigned WS_FLAG = 1024;
constexpr unsigned WS_GATE = 8192;
constexpr unsigned WS_GRAN = 16384;
constexpr unsigned WS_A2 = 262144;
constexpr unsigned A2_BYTES = 128u * KS2 * 1024u;
constexpr unsigned WS_STAMP = WS_A2 + A2_BYTES;
constexpr unsigned XS = 20480, STG = 53248;
constexpr unsigned LDS_MISC = 159744;
constexpr unsigned LDS_BYTES = 163840;
constexpr unsigned TR_STRIDE = 168, TR_WAVE = 64 * TR_STRIDE;
constexpr unsigned LDS_E = 8 * TR_WAVE, LDS_C = LDS_E + 8192;
constexpr unsigned RING_F = 15360, RING_S = 20480, RING_S0 = 4 * RING_F;
constexpr unsigned SPIN_CAP = 1u << 16;

struct Params {
    const u16 *x, *lre, *lim, *bre, *bim, *cre, *cim, *logdt, *dsk, *wg, *wu, *wd, *gw;
    float* out;
    char* ws;
};

#define WAITV(n) asm volatile("s_waitcnt vmcnt(" #n ")" ::: "memory")
#define WAITL0() asm volatile("s_waitcnt lgkmcnt(0)" ::: "memory")
#define BAR() do { asm volatile("" ::: "memory"); __builtin_amdgcn_s_barrier(); asm volatile("" ::: "memory"); } while (0)
#define SCHED() __builtin_amdgcn_sched_barrier(0)

#define STAMP(k) do {} while (0)

DI void glds16(unsigned voff, const void* sbase, unsigned ldsdst) {
    asm volatile("s_mov_b32 m0, %2\n\ts_nop 0\n\tglobal_load_lds_dwordx4 %0, %1" ::"v"(voff), "s"(sbase), "s"(ldsdst) : "memory");
}
DI u32x4 gload_sc1(unsigned voff, const void* sbase) {
    u32x4 r;
    asm volatile("global_load_dwordx4 %0, %1, %2 sc1" : "=v"(r) : "v"(voff), "s"(sbase) : "memory");
    return r;
}
DI float bf2f(unsigned v) { return __uint_as_float(v << 16); }
DI float bflo(unsigned v) { return __uint_as_float(v << 16); }
DI float bfhi(unsigned v) { return __uint_as_float(v & 0xffff0000u); }
DI unsigned pack2(float a, float b) { bf16x2 v = {(__bf16)a, (__bf16)b}; return __builtin_bit_cast(unsigned, v); }
DI float rbf(float a) { return bflo(pack2(a, a)); }
DI bf16x8 ldsr128(unsigned a) { return *(const LAS bf16x8*)(size_t)a; }
DI bf16x8 ldstr(unsigned a0, unsigned a1) {
    s16x4 lo = __builtin_amdgcn_ds_read_tr16_b64_v4i16((LAS s16x4*)(size_t)a0);
    s16x4 hi = __builtin_amdgcn_ds_read_tr16_b64_v4i16((LAS s16x4*)(size_t)a1);
    s16x8 v = __builtin_shufflevector(lo, hi, 0, 1, 2, 3, 4, 5, 6, 7);
    return __builtin_bit_cast(bf16x8, v);
}
#define MFMA(a, b, c) __builtin_amdgcn_mfma_f32_16x16x32_bf16((a), (b), (c), 0, 0, 0)
#define RLX __ATOMIC_RELAXED, __HIP_MEMORY_SCOPE_AGENT

DI void cmul(float ar, float ai, float br, float bi, float& cr, float& ci) { cr = ar * br - ai * bi; ci = ar * bi + ai * br; }

DI void ssm_params(const Params& p, int s, float& lr, float& li, float& cr, float& ci) {
    const float dt = rbf(expf(bf2f(p.logdt[s])));
    const float ar = bf2f(p.lre[s]), ai = bf2f(p.lim[s]);
    const float e = expf(dt * ar);
    float sn, cs;
    sincosf(dt * ai, &sn, &cs);
    lr = e * cs; li = e * sn;
    const float nr = lr - 1.0f, ni = li, den = ar * ar + ai * ai;
    cr = 2.0f * ((nr * ar + ni * ai) / den);
    ci = 2.0f * ((ni * ar - nr * ai) / den);
}
DI void pow20(float lr, float li, float& r, float& i) {
    float a2r, a2i, a4r, a4i, a5r, a5i, a10r, a10i;
    cmul(lr, li, lr, li, a2r, a2i); cmul(a2r, a2i, a2r, a2i, a4r, a4i); cmul(a4r, a4i, lr, li, a5r, a5i);
    cmul(a5r, a5i, a5r, a5i, a10r, a10i); cmul(a10r, a10i, a10r, a10i, r, i);
}

template <bool SSM>
DI void p1_main(const Params& p, f32x4 (&acc)[5][4], int pan, int ct, int w, int lane) {
    const int th = w >> 2, c = w & 3, t0 = pan * PM;
    unsigned xoff[3];
#pragma unroll
    for (int i = 0; i < 3; ++i) {
        const int row = 8 * (w + 8 * i) + (lane >> 3);
        int tr = row;
        if (SSM) { const int hh = row >= 80 ? 1 : 0, rr = row - 80 * hh, b = rr >> 4, rho = rr & 15; tr = 80 * hh + 20 * (rho >> 2) + 4 * b + (rho & 3); }
        const int g = min(t0 + tr, L - 1);
        xoff[i] = (unsigned)g * 1280u + (unsigned)(((lane & 7) ^ ((row >> 1) & 7)) * 16);
    }
    unsigned woff; const char* wb0; const char* wb1; unsigned wstep_i, wstep_k;
    if (!SSM) {
        const int kq = lane >> 4, swz = kq | (((w >> 2) & 1) << 2);
        int j = (lane & 15) ^ (swz << 1);
        if (ct == 13) j &= 7;
        woff = (unsigned)(4 * (w >> 1) + kq) * 3456u + (unsigned)(128 * ct + 8 * j) * 2u;
        wb0 = wb1 = (const char*)((w & 1) ? p.wu : p.wg);
        wstep_k = 64u * 3456u; wstep_i = 16u * 3456u;
    } else {
        const int r = 8 * w + (lane >> 3);
        woff = (unsigned)(128 * ct + r) * 1280u + (unsigned)(((lane & 7) ^ ((r >> 1) & 7)) * 16);
        wb0 = (const char*)p.bre; wb1 = (const char*)p.bim;
        wstep_k = 128u; wstep_i = 64u * 1280u;
    }
    const char* xb = (const char*)p.x;
    auto stageA = [&](int kt, unsigned slot) {
#pragma unroll
        for (int i = 0; i < 4; ++i) {
            const char* src = SSM ? ((i >> 1) ? wb1 : wb0) + (i & 1) * wstep_i + kt * wstep_k : wb0 + i * wstep_i + kt * wstep_k;
            glds16(woff, src, slot + XS + (w + 8 * i) * 1024);
        }
    };
    auto stageB = [&](int kt, unsigned slot) {
        glds16(xoff[0], xb + kt * 128, slot + w * 1024);
        glds16(xoff[1], xb + kt * 128, slot + (w + 8) * 1024);
        if (th == 0) glds16(xoff[2], xb + kt * 128, slot + (w + 16) * 1024);
    };
    const int rho = lane & 15, q = lane >> 4;
    const unsigned xa0 = (unsigned)(80 * th + rho) * 128u + (unsigned)((q ^ ((rho >> 1) & 7)) * 16), xa1 = xa0 ^ 64u;
    unsigned wa0, wa1;
    if (!SSM) {
        const int qq = (lane & 15) >> 2, pp = lane & 3, x = (qq << 1) | ((q & 1) << 3);
        wa0 = XS + q * 4096 + qq * 256 + (((4 * c + (pp >> 1)) ^ x) * 16) + 8 * (pp & 1);
        wa1 = XS + q * 4096 + qq * 256 + (((4 * c + 2 + (pp >> 1)) ^ x) * 16) + 8 * (pp & 1);
    } else {
        wa0 = XS + (unsigned)(32 * c + rho) * 128u + (unsigned)((q ^ ((rho >> 1) & 7)) * 16);
        wa1 = wa0 ^ 64u;
    }
    bf16x8 xf[5], wf[4];
    auto load = [&](unsigned slot, int s) {
        const unsigned xa = slot + (s ? xa1 : xa0);
        if (!SSM) {
#pragma unroll
            for (int blk = 0; blk < 4; ++blk) {
                const unsigned a = slot + ((blk >> 1) ? wa1 : wa0) + s * 16384 + (blk & 1) * 1024;
                wf[blk] = ldstr(a, a + 2048);
            }
        } else {
#pragma unroll
            for (int blk = 0; blk < 4; ++blk) wf[blk] = ldsr128(slot + (s ? wa1 : wa0) + (blk >> 1) * 16384 + (blk & 1) * 2048);
        }
#pragma unroll
        for (int b = 0; b < 5; ++b) xf[b] = ldsr128(xa + b * 2048);
    };
    auto mma = [&]() {
        __builtin_amdgcn_s_setprio(1);
#pragma unroll
        for (int b = 0; b < 5; ++b)
#pragma unroll
            for (int blk = 0; blk < 4; ++blk) acc[b][blk] = SSM ? MFMA(xf[b], wf[blk], acc[b][blk]) : MFMA(wf[blk], xf[b], acc[b][blk]);
        __builtin_amdgcn_s_setprio(0);
    };
#pragma unroll
    for (int b = 0; b < 5; ++b)
#pragma unroll
        for (int blk = 0; blk < 4; ++blk) acc[b][blk] = (f32x4){0.f, 0.f, 0.f, 0.f};

    stageA(0, 0); stageB(0, 0); stageA(1, STG); stageB(1, STG);
    if (th == 0) WAITV(7); else WAITV(6);
    BAR();
    if (th == 1) BAR();
    unsigned cur = 0, nxt2 = 2 * STG;
#define TS(i) do {} while (0)
    for (int t = 0; t < 10; ++t) {
        TS(0);
        load(cur, 0);
        if (t < 8) stageA(t + 2, nxt2);
        WAITL0(); TS(1); BAR(); SCHED(); TS(2);
        mma();
        SCHED(); TS(3); BAR(); TS(4);
        load(cur, 1);
        if (t < 8) { stageB(t + 2, nxt2); WAITL0(); TS(5); if (th == 0) WAITV(7); else WAITV(6); } else { TS(5); WAITV(0); }
        WAITL0(); TS(6); BAR(); SCHED(); TS(7);
        mma();
        SCHED(); TS(8); BAR(); TS(9);
        nxt2 = cur; cur = (cur == 2 * STG) ? 0 : cur + STG;
    }
    if (th == 0) BAR();
}

struct P2 { int i, j; };
DI void w2_stage_ffn(const Params& p, const unsigned (&vo)[5], int s, unsigned dst) {
    const char* src = (const char*)p.wd + (unsigned)s * (32u * 1280u);
#pragma unroll
    for (int u = 0; u < 5; ++u) glds16(vo[u], src, dst + u * 1024);
}
DI void w2_stage_ssm(const Params& p, const unsigned (&vo)[2], int id, unsigned dst) {
    const char* src = (const char*)(id >= 10 ? p.cim : p.cre) + (unsigned)(id >= 10 ? id - 10 : id) * 128u;
#pragma unroll
    for (int u = 0; u < 10; ++u) glds16(vo[u & 1], src + u * 10240, dst + u * 1024);
}
template <int N> DI void waitv() {
    asm volatile("s_waitcnt vmcnt(%0)" ::"n"(N) : "memory");
}
DI void tie(u32x4 (&a)[4]) { asm volatile("" : "+v"(a[0]), "+v"(a[1]), "+v"(a[2]), "+v"(a[3])); }

template <int NS> DI void p2_ffn_loop(const Params& p, f32x4 (&acc)[4][5], const unsigned (&vo)[5], const unsigned (&va)[4], int s0, unsigned ring, int lane) {
    const char* a2 = p.ws + WS_A2 + (unsigned)s0 * 1024u;
    const int g = lane >> 4, qq = (lane & 15) >> 2, pp = lane & 3;
    const unsigned lb = ring + (unsigned)(4 * g + qq) * 160u + 8u * pp;
    u32x4 A[3][4];
#pragma unroll
    for (int s = 0; s < 3; ++s)
#pragma unroll
        for (int m = 0; m < 4; ++m) A[s][m] = gload_sc1(va[m], a2 + s * 1024);
#pragma unroll
    for (int s = 0; s < NS; ++s) {
        const int rem = NS - 1 - s, r2 = rem < 2 ? rem : 2;
        if (s == 0) waitv<8>(); else if (s == 1) waitv<13>(); else if (r2 == 2) waitv<18>(); else if (r2 == 1) waitv<9>(); else waitv<0>();
        tie(A[s % 3]);
        bf16x8 wf[5];
#pragma unroll
        for (int n = 0; n < 5; ++n) { const unsigned a = lb + (s % 3) * 5120 + n * 32; wf[n] = ldstr(a, a + 2560); }
        WAITL0(); SCHED();
        if (s + 3 < NS) w2_stage_ffn(p, vo, s0 + s + 3, ring + (s % 3) * 5120);
        SCHED();
#pragma unroll
        for (int m = 0; m < 4; ++m)
#pragma unroll
            for (int n = 0; n < 5; ++n) acc[m][n] = MFMA(wf[n], __builtin_bit_cast(bf16x8, A[s % 3][m]), acc[m][n]);
        SCHED();
        if (s + 3 < NS) {
#pragma unroll
            for (int m = 0; m < 4; ++m) A[s % 3][m] = gload_sc1(va[m], a2 + (s + 3) * 1024);
        }
        SCHED();
    }
}
DI void p2_ssm_loop(const Params& p, f32x4 (&acc)[4][5], const unsigned (&vo)[2], const unsigned (&va)[4], int id0, unsigned ring, int lane) {
    const char* a2 = p.ws + WS_A2 + (unsigned)(KSF + 2 * id0) * 1024u;
    const int rho = lane & 15, q = lane >> 4;
    const unsigned lb0 = ring + (unsigned)rho * 128u + (unsigned)((q ^ ((rho >> 1) & 7)) * 16), lb1 = lb0 ^ 64u;
    u32x4 A[3][4];
#pragma unroll
    for (int s = 0; s < 3; ++s)
#pragma unroll
        for (int m = 0; m < 4; ++m) A[s][m] = gload_sc1(va[m], a2 + s * 1024);
#pragma unroll
    for (int s = 0; s < 10; ++s) {
        if (s < 2) waitv<8>(); else if (s < 8) waitv<18>(); else if (s == 8) waitv<4>(); else waitv<0>();
        tie(A[s % 3]);
        const int st = s >> 1;
        bf16x8 wf[5];
#pragma unroll
        for (int n = 0; n < 5; ++n) wf[n] = ldsr128(((s & 1) ? lb1 : lb0) + (st & 1) * 10240 + n * 2048);
        WAITL0(); SCHED();
        if ((s & 1) && st + 2 < 5) w2_stage_ssm(p, vo, id0 + st + 2, ring + (st & 1) * 10240);
        SCHED();
#pragma unroll
        for (int m = 0; m < 4; ++m)
#pragma unroll
            for (int n = 0; n < 5; ++n) acc[m][n] = MFMA(wf[n], __builtin_bit_cast(bf16x8, A[s % 3][m]), acc[m][n]);
        SCHED();
        if (s + 3 < 10) {
#pragma unroll
            for (int m = 0; m < 4; ++m) A[s % 3][m] = gload_sc1(va[m], a2 + (s + 3) * 1024);
        }
        SCHED();
    }
}

DI unsigned long long ld_flag(const char* ws, unsigned off) { return __hip_atomic_load((GAS unsigned long long*)(ws + off), RLX); }

__global__ __launch_bounds__(512, 2) void s5_swiglu_mix_kernel(Params p) {
    __shared__ __attribute__((aligned(16))) char lds[LDS_BYTES];
    const int tid = threadIdx.x, lane = tid & 63;
    const int w = __builtin_amdgcn_readfirstlane(tid >> 6);
    const int b = blockIdx.x, xcd = b & 7, r = b >> 3;
    const unsigned lds0 = (unsigned)(size_t)(LAS char*)lds;
    volatile LAS unsigned* ready = (volatile LAS unsigned*)(size_t)(lds0 + LDS_MISC);
    STAMP(0);
    const unsigned long long e_in = ld_flag(p.ws, WS_EPOCH);
    if (tid < 2) ready[tid] = 0u;
    const unsigned tag0 = (unsigned)e_in + 1u, tag1 = (unsigned)(e_in >> 32) + 1u;
    const unsigned long long tag = ((unsigned long long)tag1 << 32) | tag0;

    const __amdgpu_buffer_rsrc_t a2r = __builtin_amdgcn_make_buffer_rsrc(p.ws + WS_A2, 0, A2_BYTES, 0x00020000);

    const int n1 = 31 * xcd + r;
    int role;
    int pan = 0, ct = 0;
    unsigned flag_off;
    if (r == 31) { role = 2; flag_off = WS_FLAG + (13 * 32 + xcd) * 8; }
    else if (n1 >= 247) { role = 3; flag_off = WS_FLAG + (13 * 32 + 8) * 8; }
    else {
        const int grp = n1 / 65, rem = n1 - 65 * grp, wdt = grp == 3 ? 4 : 5;
        pan = rem / wdt; const int col = 5 * grp + rem - pan * wdt;
        role = col >= 14 ? 1 : 0; ct = role ? col - 14 : col;
        flag_off = WS_FLAG + (pan * 32 + (role ? 16 + ct : ct)) * 8;
    }
    const int ti = 8 * (xcd >> 1) + (r >> 2), tj = 4 * (xcd & 1) + (r & 3);
    unsigned vo5[5], vo2[2];
    auto w2_prefetch = [&]() {
        if (w < 4) {
#pragma unroll
            for (int u = 0; u < 5; ++u) { const int idx = 64 * u + lane, row = idx / 10, gr = idx - 10 * row; vo5[u] = (unsigned)row * 1280u + (unsigned)(80 * tj + 8 * gr) * 2u; }
            const int s0 = w < 3 ? 14 * w : 41;
#pragma unroll
            for (int s = 0; s < 3; ++s) w2_stage_ffn(p, vo5, s0 + s, lds0 + w * RING_F + s * 5120);
        } else {
#pragma unroll
            for (int u = 0; u < 2; ++u) { const int row = 8 * u + (lane >> 3); vo2[u] = (unsigned)(80 * tj + (lane >> 3)) * 1280u + (unsigned)(((lane & 7) ^ ((row >> 1) & 7)) * 16); }
#pragma unroll
            for (int s = 0; s < 2; ++s) w2_stage_ssm(p, vo2, 5 * (w - 4) + s, lds0 + RING_S0 + (w - 4) * RING_S + s * 10240);
        }
    };

    if (role == 0) {
        f32x4 acc[5][4];
        p1_main<false>(p, acc, pan, ct, w, lane);
        STAMP(1);
        w2_prefetch();
        const int th = w >> 2, c = w & 3, s2 = 4 * ct + c;
#pragma unroll
        for (int bb = 0; bb < 5; ++bb) {
            float a[8];
#pragma unroll
            for (int n = 0; n < 2; ++n)
#pragma unroll
                for (int e = 0; e < 4; ++e) {
                    const float g = acc[bb][2 * n][e], u = acc[bb][2 * n + 1][e];
                    const float sg = __builtin_amdgcn_rcpf(1.0f + __builtin_amdgcn_exp2f(g * -1.44269504088896f));
                    a[4 * n + e] = g * sg * u;
                }
            const u32x4 v = {pack2(a[0], a[1]), pack2(a[2], a[3]), pack2(a[4], a[5]), pack2(a[6], a[7])};
            const int tb = 10 * pan + 5 * th + bb;
            if (tb < 128 && s2 < KSF) __builtin_amdgcn_raw_buffer_store_b128(v, a2r, (unsigned)(tb * KS2 + s2) * 1024u + lane * 16, 0, 16);
        }
        STAMP(2);
        WAITV(0); BAR();
        if (tid == 0) __hip_atomic_store((GAS unsigned long long*)(p.ws + flag_off), tag, RLX);
        STAMP(3);
    } else if (role == 1) {
        const int th = w >> 2, c = w & 3, sg = lane & 15, q = lane >> 4;
        float lr[2], li[2], cr[2], ci[2], l20r[2], l20i[2];
#pragma unroll
        for (int n = 0; n < 2; ++n) {
            ssm_params(p, 128 * ct + 32 * c + 16 * n + sg, lr[n], li[n], cr[n], ci[n]);
            pow20(lr[n], li[n], l20r[n], l20i[n]);
        }
        float t20r = 0.f, t20i = 0.f, t160r = 0.f, t160i = 0.f;
        if (w < 2) {
            float a, bq, cc, dd;
            ssm_params(p, 128 * ct + tid, a, bq, cc, dd);
            pow20(a, bq, t20r, t20i);
            float xr, xi, yr, yi;
            cmul(t20r, t20i, t20r, t20i, xr, xi); cmul(xr, xi, xr, xi, yr, yi); cmul(yr, yi, yr, yi, t160r, t160i);
        }
        f32x4 acc[5][4];
        p1_main<true>(p, acc, pan, ct, w, lane);
        STAMP(1);
        const int seg = 4 * th + q;
#pragma unroll
        for (int n = 0; n < 2; ++n) {
            float er = 0.f, ei = 0.f;
#pragma unroll
            for (int i = 0; i < 20; ++i) {
                const float sr = acc[i >> 2][n][i & 3], si = acc[i >> 2][2 + n][i & 3];
                const float t = lr[n] * er - li[n] * ei + sr;
                ei = lr[n] * ei + li[n] * er + si; er = t;
            }
            float vr, vi; cmul(cr[n], ci[n], er, ei, vr, vi);
            *(LAS f32x2*)(size_t)(lds0 + LDS_E + (seg * 128 + 32 * c + 16 * n + sg) * 8) = (f32x2){vr, vi};
        }
        WAITL0(); BAR();
        if (w < 2 && pan < NPAN - 1) {
            float tr = 0.f, tii = 0.f;
#pragma unroll
            for (int j = 0; j < 8; ++j) {
                const f32x2 e = *(const LAS f32x2*)(size_t)(lds0 + LDS_E + (j * 128 + tid) * 8);
                const float t = t20r * tr - t20i * tii + e[0];
                tii = t20r * tii + t20i * tr + e[1]; tr = t;
            }
            const u32x4 gv = {__float_as_uint(tr), tag0, __float_as_uint(tii), tag1};
            const __amdgpu_buffer_rsrc_t gr = __builtin_amdgcn_make_buffer_rsrc(p.ws + WS_GRAN, 0, 12 * 640 * 16, 0x00020000);
            __builtin_amdgcn_raw_buffer_store_b128(gv, gr, (unsigned)(pan * 640 + 128 * ct + tid) * 16u, 0, 16);
        }
        float pr[2], pi[2], wr[2], wi[2];
#pragma unroll
        for (int n = 0; n < 2; ++n) {
            pr[n] = 0.f; pi[n] = 0.f; wr[n] = 1.f; wi[n] = 0.f;
#pragma unroll
            for (int j = 0; j < 7; ++j) {
                const f32x2 e = *(const LAS f32x2*)(size_t)(lds0 + LDS_E + (j * 128 + 32 * c + 16 * n + sg) * 8);
                const float t = l20r[n] * pr[n] - l20i[n] * pi[n] + e[0], u = l20r[n] * pi[n] + l20i[n] * pr[n] + e[1];
                float a, bq; cmul(wr[n], wi[n], l20r[n], l20i[n], a, bq);
                const bool on = j < seg;
                pr[n] = on ? t : pr[n]; pi[n] = on ? u : pi[n]; wr[n] = on ? a : wr[n]; wi[n] = on ? bq : wi[n];
            }
#pragma unroll
            for (int i = 0; i < 20; ++i) {
                const float sr = acc[i >> 2][n][i & 3], si = acc[i >> 2][2 + n][i & 3];
                acc[i >> 2][n][i & 3] = cr[n] * sr - ci[n] * si;
                acc[i >> 2][2 + n][i & 3] = cr[n] * si + ci[n] * sr;
            }
        }
        STAMP(4);
        if (w < 2) {
            float hr = 0.f, hi = 0.f;
            if (pan > 0) {
                const __amdgpu_buffer_rsrc_t gr = __builtin_amdgcn_make_buffer_rsrc(p.ws + WS_GRAN, 0, 12 * 640 * 16, 0x00020000);
                u32x4 gv[12];
                for (unsigned spins = 0;; ++spins) {
                    asm volatile("" ::: "memory");
                    bool ok = true;
#pragma unroll
                    for (int j = 0; j < 12; ++j) {
                        const int jj = j < pan ? j : pan - 1;
                        gv[j] = __builtin_amdgcn_raw_buffer_load_b128(gr, (unsigned)(jj * 640 + 128 * ct + tid) * 16u, 0, 16);
                    }
#pragma unroll
                    for (int j = 0; j < 12; ++j) ok &= (gv[j][1] == tag0) & (gv[j][3] == tag1);
                    if (__all(ok) || spins > SPIN_CAP) break;
                    __builtin_amdgcn_s_sleep(1);
                }
#pragma unroll
                for (int j = 0; j < 12; ++j) {
                    if (j < pan) {
                        const float t = t160r * hr - t160i * hi + __uint_as_float(gv[j][0]);
                        hi = t160r * hi + t160i * hr + __uint_as_float(gv[j][2]); hr = t;
                    }
                }
            }
            *(LAS f32x2*)(size_t)(lds0 + LDS_C + tid * 8) = (f32x2){hr, hi};
        }
        WAITL0(); BAR();
        STAMP(5);
        const unsigned trb = lds0 + w * TR_WAVE;
#pragma unroll
        for (int n = 0; n < 2; ++n) {
            const f32x2 cw = *(const LAS f32x2*)(size_t)(lds0 + LDS_C + (32 * c + 16 * n + sg) * 8);
            float hr = pr[n] + wr[n] * cw[0] - wi[n] * cw[1], hi = pi[n] + wr[n] * cw[1] + wi[n] * cw[0];
#pragma unroll
            for (int bb = 0; bb < 5; ++bb) {
                float o_r[4], o_i[4];
#pragma unroll
                for (int e = 0; e < 4; ++e) {
                    const float t = lr[n] * hr - li[n] * hi + acc[bb][n][e];
                    hi = lr[n] * hi + li[n] * hr + acc[bb][2 + n][e]; hr = t;
                    o_r[e] = hr; o_i[e] = -hi;
                }
                const unsigned a = trb + (16 * n + sg) * TR_STRIDE + (20 * q + 4 * bb) * 2;
                *(LAS u32x2*)(size_t)a = (u32x2){pack2(o_r[0], o_r[1]), pack2(o_r[2], o_r[3])};
                *(LAS u32x2*)(size_t)(a + 32 * TR_STRIDE) = (u32x2){pack2(o_i[0], o_i[1]), pack2(o_i[2], o_i[3])};
            }
        }
        WAITL0();
        {
            const int qq = (lane & 15) >> 2, pp = lane & 3;
            const unsigned rb = trb + (8 * q + qq) * TR_STRIDE + 8 * pp;
#pragma unroll
            for (int comp = 0; comp < 2; ++comp)
#pragma unroll
                for (int m = 0; m < 5; ++m) {
                    const unsigned a = rb + comp * 32 * TR_STRIDE + m * 32;
                    const bf16x8 v = ldstr(a, a + 4 * TR_STRIDE);
                    const int tb = 10 * pan + 5 * th + m, s2 = KSF + comp * 20 + 4 * ct + c;
                    if (tb < 128) __builtin_amdgcn_raw_buffer_store_b128(__builtin_bit_cast(u32x4, v), a2r, (unsigned)(tb * KS2 + s2) * 1024u + lane * 16, 0, 16);
                }
        }
        STAMP(2);
        WAITV(0); WAITL0(); BAR();
        if (tid == 0) __hip_atomic_store((GAS unsigned long long*)(p.ws + flag_off), tag, RLX);
        w2_prefetch();
        STAMP(3);
    } else {
        w2_prefetch();
        if (role == 2) {
            const int i16 = lane & 15, rr = lane >> 4;
            u32x4 gw[5];
#pragma unroll
            for (int k = 0; k < 5; ++k) gw[k] = *(const u32x4*)(p.gw + 8 * (i16 + 16 * k));
            float mine = 0.f;
#pragma unroll
            for (int ps = 0; ps < 8; ++ps) {
                const int row = 256 * xcd + 32 * w + 4 * ps + rr;
                float s = 0.f;
#pragma unroll
                for (int k = 0; k < 5; ++k) {
                    const u32x4 xv = *(const u32x4*)(p.x + row * D + 8 * (i16 + 16 * k));
#pragma unroll
                    for (int e = 0; e < 4; ++e) {
                        const unsigned pk = pack2(bflo(xv[e]) * bflo(gw[k][e]), bfhi(xv[e]) * bfhi(gw[k][e]));
                        s += bflo(pk); s += bfhi(pk);
                    }
                }
                s += __shfl_xor(s, 8); s += __shfl_xor(s, 4); s += __shfl_xor(s, 2); s += __shfl_xor(s, 1);
                const float z = rbf(s), g = rbf(1.0f / (1.0f + expf(-z)));
                const float gsel = __shfl(g, (lane & 3) * 16);
                if ((lane >> 2) == ps) mine = gsel;
            }
            if (lane < 32) __hip_atomic_store((GAS float*)(p.ws + WS_GATE) + 256 * xcd + 32 * w + lane, mine, RLX);
        }
        WAITV(0); WAITL0(); BAR();
        if (tid == 0) __hip_atomic_store((GAS unsigned long long*)(p.ws + flag_off), tag, RLX);
        STAMP(3);
    }

    const int p0 = (64 * ti) / PM, p1 = (64 * ti + 63) / PM;
    if (w == 0 || w == 4) {
        const int k = lane & 15, hp = (lane >> 4) & 1;
        unsigned off = WS_FLAG + ((hp ? p1 : p0) * 32 + (w ? 16 : 0) + k) * 8;
        bool need = lane < 32 && k < (w ? 5 : 14);
        if (w == 0 && lane == 32) { off = WS_FLAG + (13 * 32 + (ti >> 2)) * 8; need = true; }
        if (!need) off = WS_FLAG + (p0 * 32 + (w ? 16 : 0)) * 8;
        for (unsigned spins = 0;; ++spins) {
            const unsigned long long v = ld_flag(p.ws, off);
            if (__all(v == tag) || spins > SPIN_CAP) break;
            __builtin_amdgcn_s_sleep(1);
        }
        asm volatile("" ::: "memory");
        if (lane == 0) ready[w >> 2] = 1u;
    } else {
        while (ready[w >> 2] == 0u) __builtin_amdgcn_s_sleep(1);
    }
    asm volatile("" ::: "memory");
    STAMP(6);
    unsigned va[4];
#pragma unroll
    for (int m = 0; m < 4; ++m) va[m] = (unsigned)((4 * ti + m) * KS2) * 1024u + lane * 16;
    f32x4 acc2[4][5];
#pragma unroll
    for (int m = 0; m < 4; ++m)
#pragma unroll
        for (int n = 0; n < 5; ++n) acc2[m][n] = (f32x4){0.f, 0.f, 0.f, 0.f};
    if (w < 2) p2_ffn_loop<14>(p, acc2, vo5, va, 14 * w, lds0 + w * RING_F, lane);
    else if (w < 4) p2_ffn_loop<13>(p, acc2, vo5, va, w == 2 ? 28 : 41, lds0 + w * RING_F, lane);
    else p2_ssm_loop(p, acc2, vo2, va, 5 * (w - 4), lds0 + RING_S0 + (w - 4) * RING_S, lane);
    STAMP(7);
    if (w < 4) {
        const unsigned reg = lds0 + w * RING_F + lane * 16;
#pragma unroll
        for (int m = 0; m < 4; ++m) {
            if (m != w) {
                const int sl = m < w ? m : m - 1;
#pragma unroll
                for (int n = 0; n < 5; ++n) *(LAS f32x4*)(size_t)(reg + (sl * 5 + n) * 1024) = acc2[m][n];
            }
        }
    } else {
        const unsigned reg = lds0 + RING_S0 + (w - 4) * RING_S + lane * 16;
#pragma unroll
        for (int m = 0; m < 4; ++m)
#pragma unroll
            for (int n = 0; n < 5; ++n) *(LAS f32x4*)(size_t)(reg + (m * 5 + n) * 1024) = acc2[m][n];
    }
    if (w < 4) {
        const int q = lane >> 4, t = 64 * ti + 16 * w + (lane & 15), d0 = 80 * tj + 4 * q;
        u32x2 xv[5], dv[5];
#pragma unroll
        for (int n = 0; n < 5; ++n) { xv[n] = *(const u32x2*)(p.x + t * D + d0 + 16 * n); dv[n] = *(const u32x2*)(p.dsk + d0 + 16 * n); }
        const float g = __hip_atomic_load((GAS float*)(p.ws + WS_GATE) + t, RLX);
        const float omg = rbf(1.0f - g);
        WAITL0(); BAR();
        f32x4 own[5];
#pragma unroll
        for (int n = 0; n < 5; ++n) own[n] = w == 0 ? acc2[0][n] : w == 1 ? acc2[1][n] : w == 2 ? acc2[2][n] : acc2[3][n];
#pragma unroll
        for (int n = 0; n < 5; ++n) {
            f32x4 bsum = own[n];
#pragma unroll
            for (int o = 0; o < 4; ++o) {
                if (o != w) { const int sl = w < o ? w : w - 1; bsum += *(const LAS f32x4*)(size_t)(lds0 + o * RING_F + (sl * 5 + n) * 1024 + lane * 16); }
            }
            f32x4 y = *(const LAS f32x4*)(size_t)(lds0 + RING_S0 + (w * 5 + n) * 1024 + lane * 16);
#pragma unroll
            for (int o = 1; o < 4; ++o) y += *(const LAS f32x4*)(size_t)(lds0 + RING_S0 + o * RING_S + (w * 5 + n) * 1024 + lane * 16);
            f32x4 o4;
#pragma unroll
            for (int e = 0; e < 4; ++e) {
                const unsigned xw = xv[n][e >> 1], dw = dv[n][e >> 1];
                const float xd = rbf(((e & 1) ? bfhi(xw) : bflo(xw)) * ((e & 1) ? bfhi(dw) : bflo(dw)));
                o4[e] = g * (y[e] + xd) + rbf(omg * rbf(bsum[e]));
            }
            *(f32x4*)(p.out + t * D + d0 + 16 * n) = o4;
        }
    } else {
        WAITL0(); BAR();
        if (b == 0 && w == 7) {
            for (unsigned spins = 0;; ++spins) {
                bool ok = true;
#pragma unroll
                for (int k = 0; k < 7; ++k) {
                    const int idx = 64 * k + lane, pn = idx >> 5, sl = idx & 31;
                    const bool used = pn < 13 ? (sl < 14 || (sl >= 16 && sl < 21)) : sl < 9;
                    const unsigned long long v = ld_flag(p.ws, WS_FLAG + (used ? idx : 0) * 8);
                    ok &= v == tag;
                }
                if (__all(ok) || spins > SPIN_CAP) break;
                __builtin_amdgcn_s_sleep(1);
            }
            if (lane == 0) __hip_atomic_store((GAS unsigned long long*)(p.ws + WS_EPOCH), tag, RLX);
        }
    }
    STAMP(8);
}

}

extern "C" void kernel_launch(void* const* d_in, const int* in_sizes, int n_in, void* d_out, int out_size, void* d_ws, size_t ws_size,
                              hipStream_t stream) {
    Params p;
    p.x = (const u16*)d_in[0]; p.lre = (const u16*)d_in[1]; p.lim = (const u16*)d_in[2];
    p.bre = (const u16*)d_in[3]; p.bim = (const u16*)d_in[4]; p.cre = (const u16*)d_in[5]; p.cim = (const u16*)d_in[6];
    p.logdt = (const u16*)d_in[7]; p.dsk = (const u16*)d_in[8];
    p.wg = (const u16*)d_in[10]; p.wu = (const u16*)d_in[11]; p.wd = (const u16*)d_in[12]; p.gw = (const u16*)d_in[13];
    p.out = (float*)d_out; p.ws = (char*)d_ws;
    hipLaunchKernelGGL(s5_swiglu_mix_kernel, dim3(256), dim3(512), 0, stream, p);
}
```

```cpp
#include <hip/hip_runtime.h>
#include <stdint.h>

namespace {

typedef unsigned short u16;
typedef __bf16 bf16x8 __attribute__((ext_vector_type(8)));
typedef __bf16 bf16x2 __attribute__((ext_vector_type(2)));
typedef short s16x4 __attribute__((ext_vector_type(4)));
typedef short s16x8 __attribute__((ext_vector_type(8)));
typedef float f32x2 __attribute__((ext_vector_type(2)));
typedef float f32x4 __attribute__((ext_vector_type(4)));
typedef unsigned u32x2 __attribute__((ext_vector_type(2)));
typedef unsigned u32x4 __attribute__((ext_vector_type(4)));
#define LAS __attribute__((address_space(3)))
#define GAS __attribute__((address_space(1)))
#define DI __device__ __forceinline__

constexpr int L = 2048, D = 640, F = 1728;
constexpr int PM = 160, NPAN = 13;
constexpr int KS2 = 94, KSF = 54;
constexpr unsigned WS_EPOCH = 0;
constexpr unsigned WS_FLAG = 1024;
constexpr unsigned WS_GATE = 8192;
constexpr unsigned WS_GRAN = 16384;
constexpr unsigned WS_A2 = 262144;
constexpr unsigned A2_BYTES = 128u * KS2 * 1024u;
constexpr unsigned WS_V = WS_A2 + A2_BYTES;
constexpr unsigned V_BYTES = 128u * 24u * 1024u;
constexpr unsigned WS_VFLAG = 6144;
constexpr unsigned WS_STAMP = WS_V + V_BYTES;
constexpr unsigned XS = 20480, STG = 53248;
constexpr unsigned LDS_MISC = 159744;
constexpr unsigned LDS_BYTES = 163840;
constexpr unsigned S2 = 20480, NSLOT = 7;
constexpr unsigned TR_STRIDE = 168, TR_WAVE = 64 * TR_STRIDE;
constexpr unsigned LDS_TR = 3 * S2, LDS_E = LDS_TR + 8 * TR_WAVE;
constexpr unsigned LDS_JUNK = LDS_MISC + 2560;
constexpr unsigned LDS_TAB = LDS_MISC + 64;
static_assert(LDS_E + 8192 <= LDS_MISC && LDS_TAB + 2048 <= 163840 && NSLOT * S2 <= LDS_MISC, "LDS map");
#define NE1 9
#define NE2 3
#define WARM_T 3
constexpr unsigned SPIN_CAP = 1u << 16;

struct Params {
    const u16 *x, *lre, *lim, *bre, *bim, *cre, *cim, *logdt, *dsk, *wg, *wu, *wd, *gw;
    float* out;
    char* ws;
    u16 job[256];
    unsigned char pair[256];
};

#define WAITV(n) asm volatile("s_waitcnt vmcnt(" #n ")" ::: "memory")
#define WAITL0() asm volatile("s_waitcnt lgkmcnt(0)" ::: "memory")
#define BAR() do { asm volatile("" ::: "memory"); __builtin_amdgcn_s_barrier(); asm volatile("" ::: "memory"); } while (0)
#define SCHED() __builtin_amdgcn_sched_barrier(0)

#define CS(i) do {} while (0)
#define STAMP(k) do {} while (0)

DI void glds16(unsigned voff, const void* sbase, unsigned ldsdst) {
    asm volatile("s_mov_b32 m0, %2\n\ts_nop 0\n\tglobal_load_lds_dwordx4 %0, %1" ::"v"(voff), "s"(sbase), "s"(ldsdst) : "memory");
}
DI u32x4 gload_sc1(unsigned voff, const void* sbase) {
    u32x4 r;
    asm volatile("global_load_dwordx4 %0, %1, %2 sc1" : "=v"(r) : "v"(voff), "s"(sbase) : "memory");
    return r;
}
DI float bf2f(unsigned v) { return __uint_as_float(v << 16); }
DI float bflo(unsigned v) { return __uint_as_float(v << 16); }
DI float bfhi(unsigned v) { return __uint_as_float(v & 0xffff0000u); }
DI unsigned pack2(float a, float b) { bf16x2 v = {(__bf16)a, (__bf16)b}; return __builtin_bit_cast(unsigned, v); }
DI float rbf(float a) { return bflo(pack2(a, a)); }
DI bf16x8 ldsr128(unsigned a) { return *(const LAS bf16x8*)(size_t)a; }
DI bf16x8 ldstr(unsigned a0, unsigned a1) {
    s16x4 lo = __builtin_amdgcn_ds_read_tr16_b64_v4i16((LAS s16x4*)(size_t)a0);
    s16x4 hi = __builtin_amdgcn_ds_read_tr16_b64_v4i16((LAS s16x4*)(size_t)a1);
    s16x8 v = __builtin_shufflevector(lo, hi, 0, 1, 2, 3, 4, 5, 6, 7);
    return __builtin_bit_cast(bf16x8, v);
}
template <int N> struct IC { static constexpr int value = N; };
#define MFMA(a, b, c) __builtin_amdgcn_mfma_f32_16x16x32_bf16((a), (b), (c), 0, 0, 0)
#define RLX __ATOMIC_RELAXED, __HIP_MEMORY_SCOPE_AGENT

DI float vfma(float a, float b, float c) { return __builtin_fmaf(a, b, c); }
DI float vmul(float a, float b) { return a * b; }
DI void vfmac(float& c, float a, float b) { c = __builtin_fmaf(a, b, c); }
DI void horner2(float& ar, float& ai, float& br, float& bi, float lar, float lai, float nlai, float lbr, float lbi, float nlbi,
                float a0r, float a0i, float b0r, float b0i, float a1r, float a1i, float b1r, float b1i) {
    float t0, t1, t2, t3;
    asm("v_fma_f32 %4, %8, %0, %14\n\tv_fma_f32 %5, %8, %1, %15\n\tv_fma_f32 %6, %11, %2, %16\n\tv_fma_f32 %7, %11, %3, %17\n\t"
        "v_fmac_f32 %4, %10, %1\n\tv_fmac_f32 %5, %9, %0\n\tv_fmac_f32 %6, %13, %3\n\tv_fmac_f32 %7, %12, %2\n\t"
        "v_fma_f32 %0, %8, %4, %18\n\tv_fma_f32 %1, %8, %5, %19\n\tv_fma_f32 %2, %11, %6, %20\n\tv_fma_f32 %3, %11, %7, %21\n\t"
        "v_fmac_f32 %0, %10, %5\n\tv_fmac_f32 %1, %9, %4\n\tv_fmac_f32 %2, %13, %7\n\tv_fmac_f32 %3, %12, %6"
        : "+v"(ar), "+v"(ai), "+v"(br), "+v"(bi), "=&v"(t0), "=&v"(t1), "=&v"(t2), "=&v"(t3)
        : "v"(lar), "v"(lai), "v"(nlai), "v"(lbr), "v"(lbi), "v"(nlbi), "v"(a0r), "v"(a0i), "v"(b0r), "v"(b0i), "v"(a1r), "v"(a1i), "v"(b1r), "v"(b1i));
}
DI void cscale4(f32x4& re, f32x4& im, float cr, float ci, float nci) {
    float r0, r1, r2, r3, i0, i1, i2, i3;
    asm("v_mul_f32 %0, %8, %11\n\tv_mul_f32 %1, %8, %12\n\tv_mul_f32 %2, %8, %13\n\tv_mul_f32 %3, %8, %14\n\t"
        "v_mul_f32 %4, %8, %15\n\tv_mul_f32 %5, %8, %16\n\tv_mul_f32 %6, %8, %17\n\tv_mul_f32 %7, %8, %18\n\t"
        "v_fmac_f32 %0, %10, %15\n\tv_fmac_f32 %1, %10, %16\n\tv_fmac_f32 %2, %10, %17\n\tv_fmac_f32 %3, %10, %18\n\t"
        "v_fmac_f32 %4, %9, %11\n\tv_fmac_f32 %5, %9, %12\n\tv_fmac_f32 %6, %9, %13\n\tv_fmac_f32 %7, %9, %14"
        : "=&v"(r0), "=&v"(r1), "=&v"(r2), "=&v"(r3), "=&v"(i0), "=&v"(i1), "=&v"(i2), "=&v"(i3)
        : "v"(cr), "v"(ci), "v"(nci), "v"(re[0]), "v"(re[1]), "v"(re[2]), "v"(re[3]), "v"(im[0]), "v"(im[1]), "v"(im[2]), "v"(im[3]));
    re = (f32x4){r0, r1, r2, r3}; im = (f32x4){i0, i1, i2, i3};
}
DI void scan4(float& hr, float& hi, const f32x4& ur, const f32x4& ui, float lr, float li, float nli, u32x2& ore, u32x2& oim) {
    float r0 = ur[0], r1 = ur[1], r2 = ur[2], r3 = ur[3], i0 = ui[0], i1 = ui[1], i2 = ui[2], i3 = ui[3];
    unsigned a, b, c, d;
    asm("v_fmac_f32 %0, %12, %15\n\tv_fmac_f32 %4, %12, %16\n\tv_fmac_f32 %0, %14, %16\n\tv_fmac_f32 %4, %13, %15\n\t"
        "v_fmac_f32 %1, %12, %0\n\tv_fmac_f32 %5, %12, %4\n\tv_fmac_f32 %1, %14, %4\n\tv_fmac_f32 %5, %13, %0\n\t"
        "v_fmac_f32 %2, %12, %1\n\tv_fmac_f32 %6, %12, %5\n\tv_fmac_f32 %2, %14, %5\n\tv_fmac_f32 %6, %13, %1\n\t"
        "v_fmac_f32 %3, %12, %2\n\tv_fmac_f32 %7, %12, %6\n\tv_fmac_f32 %3, %14, %6\n\tv_fmac_f32 %7, %13, %2\n\t"
        "v_cvt_pk_bf16_f32 %8, %0, %1\n\tv_cvt_pk_bf16_f32 %9, %2, %3\n\tv_cvt_pk_bf16_f32 %10, -%4, -%5\n\tv_cvt_pk_bf16_f32 %11, -%6, -%7"
        : "+v"(r0), "+v"(r1), "+v"(r2), "+v"(r3), "+v"(i0), "+v"(i1), "+v"(i2), "+v"(i3), "=&v"(a), "=&v"(b), "=&v"(c), "=&v"(d)
        : "v"(lr), "v"(li), "v"(nli), "v"(hr), "v"(hi));
    hr = r3; hi = i3; ore = (u32x2){a, b}; oim = (u32x2){c, d};
}
DI unsigned gload_u16(unsigned voff, const void* sbase) { unsigned r; asm volatile("global_load_ushort %0, %1, %2" : "=v"(r) : "v"(voff), "s"(sbase) : "memory"); return r; }
DI void cmul(float ar, float ai, float br, float bi, float& cr, float& ci) { cr = ar * br - ai * bi; ci = ar * bi + ai * br; }

DI void ssm_params(unsigned raw_dt, unsigned raw_re, unsigned raw_im, float& lr, float& li, float& cr, float& ci) {
    const float dt = rbf(expf(bf2f(raw_dt)));
    const float ar = bf2f(raw_re), ai = bf2f(raw_im);
    const float e = expf(dt * ar);
    float sn, cs;
    sincosf(dt * ai, &sn, &cs);
    lr = e * cs; li = e * sn;
    const float nr = lr - 1.0f, ni = li, den = ar * ar + ai * ai;
    cr = 2.0f * ((nr * ar + ni * ai) / den);
    ci = 2.0f * ((ni * ar - nr * ai) / den);
}
DI void pow20(float lr, float li, float& r, float& i) {
    float a2r, a2i, a4r, a4i, a5r, a5i, a10r, a10i;
    cmul(lr, li, lr, li, a2r, a2i); cmul(a2r, a2i, a2r, a2i, a4r, a4i); cmul(a4r, a4i, lr, li, a5r, a5i);
    cmul(a5r, a5i, a5r, a5i, a10r, a10i); cmul(a10r, a10i, a10r, a10i, r, i);
}

DI void mfma_acc(f32x4& c, const bf16x8& a, const bf16x8& b) { asm volatile("v_mfma_f32_16x16x32_bf16 %0, %1, %2, %0" : "+v"(c) : "v"(a), "v"(b)); }
DI void set_m0(unsigned a, unsigned b) { asm volatile("s_add_i32 m0, %0, %1" ::"s"(a), "s"(b) : "memory", "scc"); }
template <int N> DI void set_m0i(unsigned a) { asm volatile("s_add_i32 m0, %0, %1" ::"s"(a), "n"(N) : "memory", "scc"); }
DI void glds_m0(unsigned voff, const void* sbase) { asm volatile("global_load_lds_dwordx4 %0, %1" ::"v"(voff), "s"(sbase) : "memory"); }
DI void glds_m0_half(unsigned voff, const void* sbase) {
    asm volatile("s_mov_b32 exec_hi, 0\n\tglobal_load_lds_dwordx4 %0, %1\n\ts_mov_b32 exec_hi, -1" ::"v"(voff), "s"(sbase) : "memory");
}
#define LGKM0() __builtin_amdgcn_s_waitcnt(0xC07F)
DI unsigned opaque(unsigned v) { asm volatile("" : "+v"(v)); return v; }

template <bool SSM, class H, class H2>
DI void p1_main(const Params& p, f32x4 (&acc)[5][4], int pan, int ct, int w, int lane, unsigned lds0, H pre, H2 mid) {
    const int th = w >> 2, c = w & 3, t0 = pan * PM;
    unsigned xoff[3], woff[4];
#pragma unroll
    for (int i = 0; i < 3; ++i) {
        const int row = min(20 * w + 8 * i + (lane >> 3), PM - 1);
        int tr = row;
        if (SSM) { const int hh = row >= 80 ? 1 : 0, rr = row - 80 * hh, b = rr >> 4, rho = rr & 15; tr = 80 * hh + 20 * (rho >> 2) + 4 * b + (rho & 3); }
        const int g = min(t0 + tr, L - 1);
        xoff[i] = (unsigned)g * 1280u + (unsigned)(((lane & 7) ^ ((row >> 1) & 7)) * 16);
    }
    const char* wb0; const char* wb1; unsigned wstep;
    if (!SSM) {
        const int kq = lane >> 4, swz = kq | (((w >> 2) & 1) << 2);
        int j = (lane & 15) ^ (swz << 1);
        if (ct == 13) j &= 7;
#pragma unroll
        for (int i = 0; i < 4; ++i) woff[i] = (unsigned)(4 * (w >> 1) + 16 * i + kq) * 3456u + (unsigned)(128 * ct + 8 * j) * 2u;
        wb0 = wb1 = (const char*)((w & 1) ? p.wu : p.wg);
        wstep = 64u * 3456u;
    } else {
        const int r = 8 * w + (lane >> 3);
#pragma unroll
        for (int i = 0; i < 4; ++i) woff[i] = (unsigned)(128 * ct + r + 64 * (i & 1)) * 1280u + (unsigned)(((lane & 7) ^ ((r >> 1) & 7)) * 16);
        wb0 = (const char*)p.bre; wb1 = (const char*)p.bim;
        wstep = 128u;
    }
    const char* xb = (const char*)p.x;
    const unsigned mX = lds0 + w * 2560, mW = lds0 + XS + w * 1024;
    auto stW = [&](unsigned slot, int i) { set_m0(slot, mW + i * 8192); asm volatile("s_nop 0"); glds_m0(woff[i], (SSM && i >= 2) ? wb1 : wb0); };
    auto stX = [&](unsigned slot, int i) { set_m0(slot, mX + i * 1024); asm volatile("s_nop 0"); if (i < 2) glds_m0(xoff[i], xb); else glds_m0_half(xoff[i], xb); };
    const int rho = lane & 15, q = lane >> 4;
    const unsigned xa0 = lds0 + (unsigned)(80 * th + rho) * 128u + (unsigned)((q ^ ((rho >> 1) & 7)) * 16), xa1 = xa0 ^ 64u;
    unsigned wa0, wa1;
    if (!SSM) {
        const int qq = (lane & 15) >> 2, pp = lane & 3, x = (qq << 1) | ((q & 1) << 3);
        wa0 = lds0 + XS + q * 4096 + qq * 256 + (((4 * c + (pp >> 1)) ^ x) * 16) + 8 * (pp & 1);
        wa1 = wa0 ^ 32u;
    } else {
        wa0 = lds0 + XS + (unsigned)(32 * c + rho) * 128u + (unsigned)((q ^ ((rho >> 1) & 7)) * 16);
        wa1 = wa0 ^ 64u;
    }
    bf16x8 xf[2][5], wf[2][4];
    unsigned bx, bw0, bw1;
    auto bases = [&](unsigned slot, int s) {
        bx = opaque(slot + (s ? xa1 : xa0));
        if (!SSM) { bw0 = opaque(slot + wa0); bw1 = opaque(slot + wa1); } else bw0 = opaque(slot + (s ? wa1 : wa0));
    };
    auto rdW = [&](int set, int s, int i) {
        if (!SSM) { const unsigned a = ((i >> 1) ? bw1 : bw0) + s * 16384 + (i & 1) * 1024; wf[set][i] = ldstr(a, a + 2048); }
        else wf[set][i] = ldsr128(bw0 + (i >> 1) * 16384 + (i & 1) * 2048);
    };
    auto rdX = [&](int set, int b) { xf[set][b] = ldsr128(bx + b * 2048); };
    auto mm = [&](int set, int i) {
        if (SSM) mfma_acc(acc[i >> 2][i & 3], xf[set][i >> 2], wf[set][i & 3]); else mfma_acc(acc[i >> 2][i & 3], wf[set][i & 3], xf[set][i >> 2]);
    };
#pragma unroll
    for (int b = 0; b < 5; ++b)
#pragma unroll
        for (int blk = 0; blk < 4; ++blk) acc[b][blk] = (f32x4){0.f, 0.f, 0.f, 0.f};
    asm volatile("" : "+v"(acc[0][0]), "+v"(acc[0][1]), "+v"(acc[0][2]), "+v"(acc[0][3]), "+v"(acc[1][0]), "+v"(acc[1][1]), "+v"(acc[1][2]), "+v"(acc[1][3]),
                      "+v"(acc[2][0]), "+v"(acc[2][1]), "+v"(acc[2][2]), "+v"(acc[2][3]), "+v"(acc[3][0]), "+v"(acc[3][1]), "+v"(acc[3][2]), "+v"(acc[3][3]),
                      "+v"(acc[4][0]), "+v"(acc[4][1]), "+v"(acc[4][2]), "+v"(acc[4][3]));

#define KST(i) do {} while (0)
    KST(22);
#pragma unroll
    for (int i = 0; i < 4; ++i) stW(0, i);
#pragma unroll
    for (int i = 0; i < 3; ++i) stX(0, i);
    BAR();
    wb0 += wstep; wb1 += wstep; xb += 128;
#pragma unroll
    for (int i = 0; i < 4; ++i) stW(STG, i);
#pragma unroll
    for (int i = 0; i < 3; ++i) stX(STG, i);
    wb0 += wstep; wb1 += wstep; xb += 128;
    BAR();
#pragma unroll
    for (int i = 0; i < 4; ++i) stW(2 * STG, i);
    wb0 += wstep; wb1 += wstep;
    pre();
    WAITV(11);
    BAR();
    bases(0, 0);
#pragma unroll
    for (int i = 0; i < 4; ++i) rdW(0, 0, i);
#pragma unroll
    for (int i = 0; i < 5; ++i) rdX(0, i);
    bases(0, 1);
    KST(0);
    unsigned cur = 0, nx1 = STG, nx2 = 2 * STG;
    auto body = [&](auto kind, int t) {
        constexpr int K = decltype(kind)::value;
        LGKM0(); SCHED();
#pragma unroll
        for (int i = 0; i < 20; ++i) {
            mm(0, i);
            if (i < 4) rdW(1, 1, i);
            else if (i < 9) rdX(1, i - 4);
            else if (K < 2 && (i == 10 || i == 13 || i == 16)) set_m0(nx2, mX + ((i - 10) / 3) * 1024);
            else if (K < 2 && (i == 11 || i == 14)) glds_m0(xoff[(i - 11) / 3], xb);
            else if (K < 2 && i == 17) glds_m0_half(xoff[2], xb);
            else if (K < 3 && i == 18) bases(nx1, 0);
            SCHED();
        }
        if (K < 2) { xb += 128; WAITV(7); } else if (K == 2) WAITV(0);
        KST(2 * t + 1);
        LGKM0(); BAR(); SCHED();
        KST(2 * t + 2);
#pragma unroll
        for (int i = 0; i < 20; ++i) {
            mm(1, i);
            if (K < 3) {
                if (i < 4) rdW(0, 0, i);
                else if (i < 9) rdX(0, i - 4);
                else if (K == 0 && (i == 9 || i == 11 || i == 13 || i == 15)) set_m0(cur, mW + ((i - 9) / 2) * 8192);
                else if (K == 0 && (i == 10 || i == 12 || i == 14 || i == 16)) glds_m0(woff[(i - 10) / 2], (SSM && i >= 14) ? wb1 : wb0);
                else if (i == 18) bases(nx1, 1);
            }
            SCHED();
        }
        if (K == 0) { wb0 += wstep; wb1 += wstep; }
        const unsigned o = cur; cur = nx1; nx1 = nx2; nx2 = o;
    };
#pragma unroll 1
    for (int t = 0; t < WARM_T; ++t) body(IC<0>{}, t);
    mid();
#pragma unroll 1
    for (int t = WARM_T; t < 7; ++t) body(IC<0>{}, t);
    body(IC<1>{}, 7); body(IC<2>{}, 8); body(IC<3>{}, 9);
    asm volatile("s_nop 7\n\ts_nop 7"
                 : "+v"(acc[0][0]), "+v"(acc[0][1]), "+v"(acc[0][2]), "+v"(acc[0][3]), "+v"(acc[1][0]), "+v"(acc[1][1]), "+v"(acc[1][2]), "+v"(acc[1][3]),
                   "+v"(acc[2][0]), "+v"(acc[2][1]), "+v"(acc[2][2]), "+v"(acc[2][3]), "+v"(acc[3][0]), "+v"(acc[3][1]), "+v"(acc[3][2]), "+v"(acc[3][3]),
                   "+v"(acc[4][0]), "+v"(acc[4][1]), "+v"(acc[4][2]), "+v"(acc[4][3])::"memory");
    KST(21);
}

template <int N> DI void waitv() { asm volatile("s_waitcnt vmcnt(%0)" ::"n"(N) : "memory"); }
constexpr int p2_younger(int NT, int T, int e) {
    int n = 0; bool seen = false;
    for (int t = 0; t < 3; ++t)
        for (int h = 0; h < 2; ++h) { if (seen) n += 2; if (t == T && h == e) seen = true; }
    for (int t = 0; t < NT; ++t) {
        if (t == T && e == 0) return n;
        if (t + 3 < NT) { if (seen) n += 2; if (t + 3 == T && e == 0) seen = true; }
        if (t == T && e == 1) return n;
        if (t + (int)NSLOT < NT && seen) n += 3;
        if (t + 3 < NT) { if (seen) n += 2; if (t + 3 == T && e == 1) seen = true; }
    }
    return n;
}
struct W2Src { unsigned vo[3]; unsigned m0base; };
template <bool DOWN> DI W2Src w2_src(int tj, int w, int lane, unsigned lds0) {
    W2Src r;
#pragma unroll
    for (int i = 0; i < 3; ++i) {
        if (DOWN) { const int idx = min(160 * w + 64 * i + lane, 1279), row = idx / 10, gr = idx - 10 * row; r.vo[i] = (unsigned)row * 1280u + (unsigned)(80 * tj + 8 * gr) * 2u; }
        else {
            const int row = min(20 * w + 8 * i + (lane >> 3), 159), hi = row >= 80 ? 1 : 0;
            r.vo[i] = (unsigned)(80 * tj + row - 80 * hi) * 1280u + (unsigned)(128 * hi) + (unsigned)(((lane & 7) ^ ((row >> 1) & 7)) * 16);
        }
    }
    r.m0base = lds0 + w * 2560;
    return r;
}
template <bool DOWN> DI const char* w2_base(const Params& p, int T, int w) {
    if (DOWN) return (const char*)p.wd + (unsigned)T * (128u * 1280u) - ((T == 13 && w >= 4) ? 64u * 1280u : 0u);
    return (const char*)(T >= 5 ? p.cim : p.cre) + (unsigned)(T >= 5 ? T - 5 : T) * 256u;
}
template <bool DOWN> DI void w2_piece(const Params& p, const W2Src& s, int T, int w, int i) {
    set_m0(s.m0base, (unsigned)((T % NSLOT) * S2 + i * 1024));
    asm volatile("s_nop 0");
    if (i < 2) glds_m0(s.vo[i], w2_base<DOWN>(p, T, w)); else glds_m0_half(s.vo[i], w2_base<DOWN>(p, T, w));
}
DI void l2_warm(const Params& p, int b, int w, int lane, unsigned lds0) {
    const int cg = b & 1, l = ((b >> 3) * 8 + w) * 64 + lane;
    const char* a;
    if (l < 8640) { const int row = l / 5; a = (const char*)p.wd + row * 1280 + 640 * cg + 128 * (l - 5 * row); }
    else { const int m = min(l - 8640, 6399), im = m >= 3200 ? 1 : 0, r = m - 3200 * im, row = r / 10; a = (const char*)(im ? p.cim : p.cre) + (320 * cg + row) * 1280 + 128 * (r - 10 * row); }
    asm volatile("s_mov_b32 m0, %1\n\ts_nop 0\n\tglobal_load_lds_dword %0, off" ::"v"(a), "s"(lds0 + LDS_JUNK) : "memory");
}
DI void tie2(u32x4& a, u32x4& b) { asm volatile("" : "+v"(a), "+v"(b)); }

#define CSARG
#define CSPASS
template <bool DOWN> DI void p2_loop(const Params& p, f32x4 (&acc)[2][5], const W2Src& src, int ti, int w, int lane, unsigned lds0 CSARG) {
    constexpr int NT = DOWN ? 14 : 10;
    const int tg = w & 3, kh = w >> 2;
    const char* a2 = p.ws + WS_A2 + (unsigned)((DOWN ? 0 : KSF) + 2 * kh) * 1024u;
    unsigned va[2];
#pragma unroll
    for (int m = 0; m < 2; ++m) va[m] = (unsigned)((8 * ti + 2 * tg + (m ^ kh)) * KS2) * 1024u + lane * 16;
    u32x4 A[3][2][2];
    auto ldA = [&](int T, int e) {
#pragma unroll
        for (int m = 0; m < 2; ++m) A[T % 3][e][m] = gload_sc1(va[m], a2 + (4 * T + e) * 1024);
    };
#pragma unroll
    for (int T = 0; T < 3; ++T) { ldA(T, 0); ldA(T, 1); }
    unsigned lb0, lb1;
    if (DOWN) { const int g = lane >> 4, qq = (lane & 15) >> 2, pp = lane & 3; lb0 = lds0 + (unsigned)(64 * kh + 4 * g + qq) * 160u + 8u * pp; lb1 = lb0 + 5120; }
    else { const int rho = lane & 15, q = lane >> 4; lb0 = lds0 + (unsigned)(80 * kh + rho) * 128u + (unsigned)((q ^ ((rho >> 1) & 7)) * 16); lb1 = lb0 ^ 64u; }
    bf16x8 wf[2][5];
    unsigned rb;
    auto rdW = [&](int set, int n) { if (DOWN) wf[set][n] = ldstr(rb + n * 32, rb + n * 32 + 2560); else wf[set][n] = ldsr128(rb + n * 2048); };
#pragma unroll
    for (int m = 0; m < 2; ++m)
#pragma unroll
        for (int n = 0; n < 5; ++n) acc[m][n] = (f32x4){0.f, 0.f, 0.f, 0.f};
    asm volatile("" : "+v"(acc[0][0]), "+v"(acc[0][1]), "+v"(acc[0][2]), "+v"(acc[0][3]), "+v"(acc[0][4]), "+v"(acc[1][0]), "+v"(acc[1][1]), "+v"(acc[1][2]),
                      "+v"(acc[1][3]), "+v"(acc[1][4]));
    waitv<12>(); BAR();
    rb = opaque(lb0);
#pragma unroll
    for (int n = 0; n < 5; ++n) rdW(0, n);
#define DBGNAN(v) do {} while (0)
    auto step = [&](auto tc) {
        constexpr int T = decltype(tc)::value;
        const bool live = !(DOWN && T == 13) || kh == 0;
        waitv<p2_younger(NT, T, 0)>(); tie2(A[T % 3][0][0], A[T % 3][0][1]);
        rb = opaque(lb1 + (T % NSLOT) * S2);
        LGKM0(); SCHED();
        if (live) {
#pragma unroll
            for (int i = 0; i < 10; ++i) {
                mfma_acc(acc[i / 5][i % 5], wf[0][i % 5], __builtin_bit_cast(bf16x8, A[T % 3][0][i / 5]));
                if (i < 5) rdW(1, i);
                SCHED();
            }
        }
        DBGNAN(2 * T + 1);
        if (T + 3 < NT) ldA(T + 3, 0);
        waitv<p2_younger(NT, T, 1)>(); tie2(A[T % 3][1][0], A[T % 3][1][1]);
        LGKM0(); BAR(); SCHED();
        if (T + 1 < NT) rb = opaque(lb0 + ((T + 1) % NSLOT) * S2);
        if (live) {
#pragma unroll
            for (int i = 0; i < 10; ++i) {
                mfma_acc(acc[i / 5][i % 5], wf[1][i % 5], __builtin_bit_cast(bf16x8, A[T % 3][1][i / 5]));
                if (T + 1 < NT && i < 5) rdW(0, i);
                if (T + (int)NSLOT < NT && i >= 5 && i < 8) w2_piece<DOWN>(p, src, T + NSLOT, w, i - 5);
                SCHED();
            }
        }
        DBGNAN(2 * T + 2);
        if (T + 3 < NT) ldA(T + 3, 1);
        SCHED();
    };
    step(IC<0>{}); step(IC<1>{}); step(IC<2>{}); step(IC<3>{}); step(IC<4>{}); step(IC<5>{}); step(IC<6>{}); step(IC<7>{}); step(IC<8>{}); step(IC<9>{});
    if (DOWN) { step(IC<10>{}); step(IC<11>{}); step(IC<12>{}); step(IC<13>{}); }
    asm volatile("s_nop 7\n\ts_nop 7"
                 : "+v"(acc[0][0]), "+v"(acc[0][1]), "+v"(acc[0][2]), "+v"(acc[0][3]), "+v"(acc[0][4]), "+v"(acc[1][0]), "+v"(acc[1][1]), "+v"(acc[1][2]),
                   "+v"(acc[1][3]), "+v"(acc[1][4])::"memory");
    const unsigned xw = lds0 + w * 5120 + lane * 16, xr = lds0 + (w ^ 4) * 5120 + lane * 16;
#pragma unroll
    for (int n = 0; n < 5; ++n) *(LAS f32x4*)(size_t)(xw + n * 1024) = acc[1][n];
    WAITL0(); BAR();
#pragma unroll
    for (int n = 0; n < 5; ++n) acc[0][n] += *(const LAS f32x4*)(size_t)(xr + n * 1024);
}
static_assert(p2_younger(10, 0, 0) == 10 && p2_younger(10, 0, 1) == 10 && p2_younger(14, 3, 0) == 19 && p2_younger(14, 3, 1) == 16 && p2_younger(10, 9, 1) == 0, "vmcnt schedule");

DI unsigned long long ld_flag(const char* ws, unsigned off) { return __hip_atomic_load((GAS unsigned long long*)(ws + off), RLX); }

DI void spin_flags(const char* ws, unsigned off, unsigned long long tag) {
    for (unsigned spins = 0;; ++spins) {
        if (__all(ld_flag(ws, off) == tag) || spins > SPIN_CAP) break;
        __builtin_amdgcn_s_sleep(1);
    }
    asm volatile("" ::: "memory");
}

__global__ __launch_bounds__(512, 2) void s5_swiglu_mix_kernel(Params p) {
    __shared__ __attribute__((aligned(1024))) char lds[LDS_BYTES];
    const int tid = threadIdx.x, lane = tid & 63;
    const int w = __builtin_amdgcn_readfirstlane(tid >> 6);
    const int b = blockIdx.x;
    const unsigned lds0 = (unsigned)(size_t)(LAS char*)lds;
    CS(0);
    STAMP(0);
    const unsigned long long e_in = ld_flag(p.ws, WS_EPOCH);
    const unsigned tag0 = (unsigned)e_in + 1u, tag1 = (unsigned)(e_in >> 32) + 1u;
    const unsigned long long tag = ((unsigned long long)tag1 << 32) | tag0;
    const __amdgpu_buffer_rsrc_t a2r = __builtin_amdgcn_make_buffer_rsrc(p.ws + WS_A2, 0, A2_BYTES, 0x00020000);

    const unsigned job = p.job[b];
    const int role = job & 3;
    const int pan = (job >> 2) & 15, ct = (job >> 6) & 15;
    const bool down = (job >> 10) & 1;
    const int pair = p.pair[b], ti = pair >> 3, tj = pair & 7;
    const unsigned flag_off = WS_FLAG + (role == 0 ? pan * 32 + ct : role == 1 ? pan * 32 + 16 + ct : role == 2 ? 13 * 32 + pan : 13 * 32 + 8) * 8;
    const W2Src src = down ? w2_src<true>(tj, w, lane, lds0) : w2_src<false>(tj, w, lane, lds0);

    auto warm = [&]() { l2_warm(p, b, w, lane, lds0); };

    if (role == 0) {
        f32x4 acc[5][4];
        p1_main<false>(p, acc, pan, ct, w, lane, lds0, []() {}, warm);
        STAMP(1); CS(1);
        const int th = w >> 2, c = w & 3, s2 = 4 * ct + c;
        auto epi = [&](auto dn) {
            constexpr bool DN = decltype(dn)::value != 0;
            auto w2_pc = [&](int k) { w2_piece<DN>(p, src, k / 3, w, k % 3); };
#pragma unroll
            for (int bb = 0; bb < 5; ++bb) {
                float a[8];
#pragma unroll
                for (int k = 0; k < 8; ++k) a[k] = __builtin_amdgcn_exp2f(acc[bb][2 * (k >> 2)][k & 3] * -1.44269504088896f);
                if (2 * bb < NE1) w2_pc(2 * bb);
                SCHED();
#pragma unroll
                for (int k = 0; k < 8; ++k) a[k] = __builtin_amdgcn_rcpf(1.0f + a[k]);
                if (2 * bb + 1 < NE1) w2_pc(2 * bb + 1);
                SCHED();
#pragma unroll
                for (int k = 0; k < 8; ++k) a[k] = acc[bb][2 * (k >> 2)][k & 3] * a[k] * acc[bb][2 * (k >> 2) + 1][k & 3];
                const u32x4 v = {pack2(a[0], a[1]), pack2(a[2], a[3]), pack2(a[4], a[5]), pack2(a[6], a[7])};
                const int tb = 10 * pan + 5 * th + bb;
                if (tb < 128 && s2 < KSF) __builtin_amdgcn_raw_buffer_store_b128(v, a2r, (unsigned)(tb * KS2 + s2) * 1024u + lane * 16, 0, 16);
                SCHED();
            }
            STAMP(2); CS(2);
#pragma unroll
            for (int k = NE1; k < NE1 + NE2; ++k) w2_pc(k);
            waitv<NE2>(); CS(3); BAR(); CS(4);
            if (w == 0) __hip_atomic_store((GAS unsigned long long*)(p.ws + flag_off), tag, RLX);
#pragma unroll
            for (int k = NE1 + NE2; k < 3 * (int)NSLOT; ++k) w2_pc(k);
        };
        if (down) epi(IC<1>{}); else epi(IC<0>{});
        STAMP(3);
    } else if (role == 1) {
        const int th = w >> 2, c = w & 3, sg = lane & 15, q = lane >> 4;
        float t20r = 0.f, t20i = 0.f, t160r = 0.f, t160i = 0.f;
        unsigned raw[3] = {0u, 0u, 0u};
        if (w < 2) { const unsigned st = (unsigned)(128 * ct + tid) * 2u; raw[0] = gload_u16(st, p.logdt); raw[1] = gload_u16(st, p.lre); raw[2] = gload_u16(st, p.lim); }
        f32x4 acc[5][4];
        p1_main<true>(p, acc, pan, ct, w, lane, lds0, [&]() {
            if (w < 2) {
                WAITV(18);
                asm volatile("" : "+v"(raw[0]), "+v"(raw[1]), "+v"(raw[2]));
                float a, bq, cc, dd, xr, xi, yr, yi;
                ssm_params(raw[0], raw[1], raw[2], a, bq, cc, dd);
                *(LAS f32x4*)(size_t)(lds0 + LDS_TAB + tid * 16) = (f32x4){a, bq, cc, dd};
                pow20(a, bq, t20r, t20i);
                cmul(t20r, t20i, t20r, t20i, xr, xi); cmul(xr, xi, xr, xi, yr, yi); cmul(yr, yi, yr, yi, t160r, t160i);
                WAITL0();
            }
        }, warm);
        STAMP(1); CS(1);
        float lr[2], li[2], cr[2], ci[2];
#pragma unroll
        for (int n = 0; n < 2; ++n) {
            const f32x4 t = *(const LAS f32x4*)(size_t)(lds0 + LDS_TAB + (32 * c + 16 * n + sg) * 16);
            lr[n] = t[0]; li[n] = t[1]; cr[n] = t[2]; ci[n] = t[3];
        }
        const int seg = 4 * th + q;
        const unsigned ea = lds0 + LDS_E + (seg * 128 + 32 * c + sg) * 8;
        const float nli[2] = {-li[0], -li[1]}, nci[2] = {-ci[0], -ci[1]};
        {
            float er0 = 0.f, ei0 = 0.f, er1 = 0.f, ei1 = 0.f;
#pragma unroll
            for (int i = 0; i < 20; i += 2)
                horner2(er0, ei0, er1, ei1, lr[0], li[0], nli[0], lr[1], li[1], nli[1],
                        acc[i >> 2][0][i & 3], acc[i >> 2][2][i & 3], acc[i >> 2][1][i & 3], acc[i >> 2][3][i & 3],
                        acc[i >> 2][0][(i & 3) + 1], acc[i >> 2][2][(i & 3) + 1], acc[i >> 2][1][(i & 3) + 1], acc[i >> 2][3][(i & 3) + 1]);
            *(LAS f32x2*)(size_t)ea = (f32x2){cr[0] * er0 - ci[0] * ei0, cr[0] * ei0 + ci[0] * er0};
            *(LAS f32x2*)(size_t)(ea + 128) = (f32x2){cr[1] * er1 - ci[1] * ei1, cr[1] * ei1 + ci[1] * er1};
        }
        CS(5); WAITL0(); BAR(); CS(6);
        float pr[8], pi[8];
        if (w < 2) {
            float tr = 0.f, tii = 0.f;
#pragma unroll
            for (int j = 0; j < 8; ++j) {
                const f32x2 e = *(const LAS f32x2*)(size_t)(lds0 + LDS_E + (j * 128 + tid) * 8);
                pr[j] = tr; pi[j] = tii;
                const float t = t20r * tr - t20i * tii + e[0];
                tii = t20r * tii + t20i * tr + e[1]; tr = t;
            }
            if (pan < NPAN - 1) {
                const u32x4 gv = {__float_as_uint(tr), tag0, __float_as_uint(tii), tag1};
                const __amdgpu_buffer_rsrc_t gr = __builtin_amdgcn_make_buffer_rsrc(p.ws + WS_GRAN, 0, 12 * 640 * 16, 0x00020000);
                __builtin_amdgcn_raw_buffer_store_b128(gv, gr, (unsigned)(pan * 640 + 128 * ct + tid) * 16u, 0, 16);
            }
        }
        CS(7);
#pragma unroll
        for (int k = 0; k < 10; ++k) {
            const int n = k & 1, bb = k >> 1;
            cscale4(acc[bb][n], acc[bb][2 + n], cr[n], ci[n], nci[n]);
            if (k < 9 && w >= 2) w2_piece<false>(p, src, k / 3, w, k % 3);
            SCHED();
        }
        STAMP(4); CS(8);
        if (w < 2) {
            float hr = 0.f, hi = 0.f;
            if (pan > 0) {
                const __amdgpu_buffer_rsrc_t gr = __builtin_amdgcn_make_buffer_rsrc(p.ws + WS_GRAN, 0, 12 * 640 * 16, 0x00020000);
                u32x4 gv[12];
                for (unsigned spins = 0;; ++spins) {
                    asm volatile("" ::: "memory");
                    bool ok = true;
#pragma unroll
                    for (int j = 0; j < 12; ++j) {
                        const int jj = j < pan ? j : pan - 1;
                        gv[j] = __builtin_amdgcn_raw_buffer_load_b128(gr, (unsigned)(jj * 640 + 128 * ct + tid) * 16u, 0, 16);
                    }
#pragma unroll
                    for (int j = 0; j < 12; ++j) ok &= (gv[j][1] == tag0) & (gv[j][3] == tag1);
                    if (__all(ok) || spins > SPIN_CAP) break;
                    __builtin_amdgcn_s_sleep(1);
                }
#pragma unroll
                for (int j = 0; j < 12; ++j) {
                    if (j < pan) {
                        const float t = t160r * hr - t160i * hi + __uint_as_float(gv[j][0]);
                        hi = t160r * hi + t160i * hr + __uint_as_float(gv[j][2]); hr = t;
                    }
                }
            }
#pragma unroll
            for (int j = 0; j < 8; ++j) {
                *(LAS f32x2*)(size_t)(lds0 + LDS_E + (j * 128 + tid) * 8) = (f32x2){pr[j] + hr, pi[j] + hi};
                const float t = t20r * hr - t20i * hi;
                hi = t20r * hi + t20i * hr; hr = t;
            }
#pragma unroll
            for (int k = 0; k < 9; ++k) w2_piece<false>(p, src, k / 3, w, k % 3);
        }
        CS(9); WAITL0(); BAR();
        STAMP(5); CS(10);
        const unsigned trb = lds0 + LDS_TR + w * TR_WAVE;
        {
            f32x2 cw[2];
#pragma unroll
            for (int n = 0; n < 2; ++n) cw[n] = *(const LAS f32x2*)(size_t)(ea + n * 128);
            float hr[2] = {cw[0][0], cw[1][0]}, hi[2] = {cw[0][1], cw[1][1]};
            const unsigned ta = trb + sg * TR_STRIDE + 40 * q;
#pragma unroll
            for (int bb = 0; bb < 5; ++bb) {
#pragma unroll
                for (int n = 0; n < 2; ++n) {
                    u32x2 ore, oim;
                    scan4(hr[n], hi[n], acc[bb][n], acc[bb][2 + n], lr[n], li[n], nli[n], ore, oim);
                    const unsigned a = ta + 16 * n * TR_STRIDE + 8 * bb;
                    *(LAS u32x2*)(size_t)a = ore;
                    *(LAS u32x2*)(size_t)(a + 32 * TR_STRIDE) = oim;
                }
            }
        }
        WAITL0(); CS(11);
        {
            const int qq = (lane & 15) >> 2, pp = lane & 3;
            const unsigned rb = trb + (8 * q + qq) * TR_STRIDE + 8 * pp;
#pragma unroll
            for (int comp = 0; comp < 2; ++comp)
#pragma unroll
                for (int m = 0; m < 5; ++m) {
                    const unsigned a = rb + comp * 32 * TR_STRIDE + m * 32;
                    const bf16x8 v = ldstr(a, a + 4 * TR_STRIDE);
                    const int tb = 10 * pan + 5 * th + m, s2 = KSF + comp * 20 + 4 * ct + c;
                    if (tb < 128) __builtin_amdgcn_raw_buffer_store_b128(__builtin_bit_cast(u32x4, v), a2r, (unsigned)(tb * KS2 + s2) * 1024u + lane * 16, 0, 16);
                }
        }
        STAMP(2); CS(2);
        WAITV(0); CS(3); WAITL0(); BAR(); CS(4);
        if (w == 0) __hip_atomic_store((GAS unsigned long long*)(p.ws + flag_off), tag, RLX);
#pragma unroll
        for (int k = 9; k < 3 * (int)NSLOT; ++k) w2_piece<false>(p, src, k / 3, w, k % 3);
        STAMP(3);
    } else {
        warm();
        if (role == 2) {
            const int i16 = lane & 15, rr = lane >> 4;
            u32x4 gw[5];
#pragma unroll
            for (int k = 0; k < 5; ++k) gw[k] = *(const u32x4*)(p.gw + 8 * (i16 + 16 * k));
            float mine = 0.f;
#pragma unroll
            for (int hf = 0; hf < 2; ++hf) {
                u32x4 xv[4][5];
#pragma unroll
                for (int ps = 0; ps < 4; ++ps)
#pragma unroll
                    for (int k = 0; k < 5; ++k) xv[ps][k] = *(const u32x4*)(p.x + (256 * pan + 32 * w + 16 * hf + 4 * ps + rr) * D + 8 * (i16 + 16 * k));
#pragma unroll
                for (int ps = 0; ps < 4; ++ps) {
                    float s = 0.f;
#pragma unroll
                    for (int k = 0; k < 5; ++k)
#pragma unroll
                        for (int e = 0; e < 4; ++e) {
                            const unsigned pk = pack2(bflo(xv[ps][k][e]) * bflo(gw[k][e]), bfhi(xv[ps][k][e]) * bfhi(gw[k][e]));
                            s += bflo(pk); s += bfhi(pk);
                        }
                    s += __shfl_xor(s, 8); s += __shfl_xor(s, 4); s += __shfl_xor(s, 2); s += __shfl_xor(s, 1);
                    const float z = rbf(s), g = rbf(1.0f / (1.0f + expf(-z)));
                    const float gsel = __shfl(g, (lane & 3) * 16);
                    if ((lane >> 2) == 4 * hf + ps) mine = gsel;
                }
            }
            if (lane < 32) __hip_atomic_store((GAS float*)(p.ws + WS_GATE) + 256 * pan + 32 * w + lane, mine, RLX);
        }
        WAITV(0); WAITL0(); BAR();
        if (w == 0) __hip_atomic_store((GAS unsigned long long*)(p.ws + flag_off), tag, RLX);
        if (down) {
#pragma unroll
            for (int k = 0; k < 3 * (int)NSLOT; ++k) w2_piece<true>(p, src, k / 3, w, k % 3);
        } else {
#pragma unroll
            for (int k = 0; k < 3 * (int)NSLOT; ++k) w2_piece<false>(p, src, k / 3, w, k % 3);
        }
        STAMP(3);
    }
    CS(12);
    if (w == 0) {
        const int p0 = (128 * ti) / PM, p1 = (128 * ti + 127) / PM, k = lane & 15, n = down ? 14 : 5;
        spin_flags(p.ws, WS_FLAG + (((lane >> 4) & 1 ? p1 : p0) * 32 + (down ? 0 : 16) + (k < n ? k : 0)) * 8, tag);
    }
    BAR();
    STAMP(6); CS(13);
    f32x4 acc2[2][5];
    if (down) p2_loop<true>(p, acc2, src, ti, w, lane, lds0 CSPASS); else p2_loop<false>(p, acc2, src, ti, w, lane, lds0 CSPASS);
    STAMP(7); CS(14);
    const int tb = 8 * ti + 2 * (w & 3) + (w >> 2), t = 16 * tb + (lane & 15), q = lane >> 4;
    const __amdgpu_buffer_rsrc_t vr = __builtin_amdgcn_make_buffer_rsrc(p.ws + WS_V, 0, V_BYTES, 0x00020000);
    const unsigned voff = (unsigned)(pair * 8 + (tb & 7)) * 3072u + lane * 16;
    spin_flags(p.ws, WS_FLAG + (13 * 32 + (t >> 8)) * 8, tag);
    const float g = __hip_atomic_load((GAS float*)(p.ws + WS_GATE) + t, RLX);
    if (down) {
        const float omg = rbf(1.0f - g);
        unsigned pk[12];
#pragma unroll
        for (int n = 0; n < 5; ++n) {
            pk[2 * n] = pack2(omg * rbf(acc2[0][n][0]), omg * rbf(acc2[0][n][1]));
            pk[2 * n + 1] = pack2(omg * rbf(acc2[0][n][2]), omg * rbf(acc2[0][n][3]));
        }
        pk[10] = pk[11] = 0u;
#pragma unroll
        for (int k = 0; k < 3; ++k) __builtin_amdgcn_raw_buffer_store_b128((u32x4){pk[4 * k], pk[4 * k + 1], pk[4 * k + 2], pk[4 * k + 3]}, vr, voff + k * 1024, 0, 16);
        WAITV(0); BAR();
        if (w == 0) __hip_atomic_store((GAS unsigned long long*)(p.ws + WS_VFLAG + pair * 8), tag, RLX);
    } else {
        const int d0 = 80 * tj + 4 * q;
        u32x2 xv[5], dv[5];
#pragma unroll
        for (int n = 0; n < 5; ++n) { xv[n] = *(const u32x2*)(p.x + t * D + d0 + 16 * n); dv[n] = *(const u32x2*)(p.dsk + d0 + 16 * n); }
        CS(15);
        if (w == 0) spin_flags(p.ws, WS_VFLAG + pair * 8, tag);
        BAR();
        CS(16);
        u32x4 vv[3];
#pragma unroll
        for (int k = 0; k < 3; ++k) vv[k] = __builtin_amdgcn_raw_buffer_load_b128(vr, voff + k * 1024, 0, 16);
#pragma unroll
        for (int n = 0; n < 5; ++n) {
            f32x4 o4;
#pragma unroll
            for (int e = 0; e < 4; ++e) {
                const unsigned xw = xv[n][e >> 1], dw = dv[n][e >> 1], vw = vv[(2 * n + (e >> 1)) >> 2][(2 * n + (e >> 1)) & 3];
                const float xd = rbf(((e & 1) ? bfhi(xw) : bflo(xw)) * ((e & 1) ? bfhi(dw) : bflo(dw)));
                o4[e] = g * (acc2[0][n][e] + xd) + ((e & 1) ? bfhi(vw) : bflo(vw));
            }
            *(f32x4*)(p.out + t * D + d0 + 16 * n) = o4;
        }
    }
    if (b == 0 && w == 7) {
        for (unsigned spins = 0;; ++spins) {
            bool ok = true;
#pragma unroll
            for (int k = 0; k < 7; ++k) {
                const int idx = 64 * k + lane, pn = idx >> 5, sl = idx & 31;
                const bool used = pn < 13 ? (sl < 14 || (sl >= 16 && sl < 21)) : sl < 9;
                ok &= ld_flag(p.ws, WS_FLAG + (used ? idx : 0) * 8) == tag;
            }
            if (__all(ok) || spins > SPIN_CAP) break;
            __builtin_amdgcn_s_sleep(1);
        }
        if (lane == 0) __hip_atomic_store((GAS unsigned long long*)(p.ws + WS_EPOCH), tag, RLX);
    }
    STAMP(8); CS(17);
}

void make_jobs(Params& p) {
    static const int cols[19] = {0, 1, 2, 3, 16, 4, 5, 6, 7, 17, 8, 9, 10, 11, 18, 12, 13, 19, 20};
    for (int x = 0; x < 8; ++x) {
        int role[32], pan[32], ct[32];
        for (int r = 0; r < 32; ++r) {
            const int n1 = 31 * x + r;
            if (r == 31) { role[r] = 2; pan[r] = x; ct[r] = 0; }
            else if (n1 >= 247) { role[r] = 3; pan[r] = 0; ct[r] = 0; }
            else {
                const int grp = n1 / 65, rem = n1 - 65 * grp, wdt = grp == 3 ? 4 : 5, pn = rem / wdt, col = cols[5 * grp + rem - pn * wdt];
                role[r] = col >= 16 ? 1 : 0; pan[r] = pn; ct[r] = col >= 16 ? col - 16 : col;
            }
        }
        int isout[32], nout = 0;
        for (int r = 0; r < 32; ++r) { isout[r] = role[r] == 1; nout += isout[r]; }
        for (int r = 0; r < 32 && nout < 16; ++r) if (role[r] == 0) { isout[r] = 1; ++nout; }
        int no = 0, nd = 0;
        for (int r = 0; r < 32; ++r) {
            const int k = isout[r] ? no++ : nd++;
            const int ti = 4 * (x >> 1) + (k >> 2), tj = 4 * (x & 1) + (k & 3), b = 8 * r + x;
            p.job[b] = (u16)(role[r] | (pan[r] << 2) | (ct[r] << 6) | ((isout[r] ? 0 : 1) << 10));
            p.pair[b] = (unsigned char)(ti * 8 + tj);
        }
    }
}

}

extern "C" void kernel_launch(void* const* d_in, const int* in_sizes, int n_in, void* d_out, int out_size, void* d_ws, size_t ws_size,
                              hipStream_t stream) {
    Params p;
    p.x = (const u16*)d_in[0]; p.lre = (const u16*)d_in[1]; p.lim = (const u16*)d_in[2];
    p.bre = (const u16*)d_in[3]; p.bim = (const u16*)d_in[4]; p.cre = (const u16*)d_in[5]; p.cim = (const u16*)d_in[6];
    p.logdt = (const u16*)d_in[7]; p.dsk = (const u16*)d_in[8];
    p.wg = (const u16*)d_in[10]; p.wu = (const u16*)d_in[11]; p.wd = (const u16*)d_in[12]; p.gw = (const u16*)d_in[13];
    p.out = (float*)d_out; p.ws = (char*)d_ws;
    make_jobs(p);
    hipLaunchKernelGGL(s5_swiglu_mix_kernel, dim3(256), dim3(512), 0, stream, p);
}
```

```cpp
#include <hip/hip_runtime.h>
#include <stdint.h>

namespace {

typedef unsigned short u16;
typedef __bf16 bf16x8 __attribute__((ext_vector_type(8)));
typedef __bf16 bf16x2 __attribute__((ext_vector_type(2)));
typedef short s16x4 __attribute__((ext_vector_type(4)));
typedef short s16x8 __attribute__((ext_vector_type(8)));
typedef float f32x2 __attribute__((ext_vector_type(2)));
typedef float f32x4 __attribute__((ext_vector_type(4)));
typedef unsigned u32x2 __attribute__((ext_vector_type(2)));
typedef unsigned u32x4 __attribute__((ext_vector_type(4)));
#define LAS __attribute__((address_space(3)))
#define GAS __attribute__((address_space(1)))
#define DI __device__ __forceinline__

constexpr int L = 2048, D = 640, F = 1728;
constexpr int PM = 160, NPAN = 13;
constexpr int KS2 = 94, KSF = 54;
constexpr unsigned WS_EPOCH = 0;
constexpr unsigned WS_FLAG = 1024;
constexpr unsigned WS_GATE = 8192;
constexpr unsigned WS_GRAN = 16384;
constexpr unsigned WS_A2 = 262144;
constexpr unsigned A2_BYTES = 128u * KS2 * 1024u;
constexpr unsigned WS_V = WS_A2 + A2_BYTES;
constexpr unsigned V_BYTES = 128u * 24u * 1024u;
constexpr unsigned WS_VFLAG = 6144;
constexpr unsigned WS_STAMP = WS_V + V_BYTES;
constexpr unsigned XS = 20480, STG = 53248;
constexpr unsigned LDS_MISC = 159744;
constexpr unsigned LDS_BYTES = 163840;
constexpr unsigned S2 = 20480, NSLOT = 7;
constexpr unsigned TR_STRIDE = 168, TR_WAVE = 64 * TR_STRIDE;
constexpr unsigned LDS_TR = 3 * S2, LDS_E = LDS_TR + 8 * TR_WAVE;
constexpr unsigned LDS_JUNK = LDS_MISC + 2560;
constexpr unsigned LDS_TAB = LDS_MISC + 64;
static_assert(LDS_E + 8192 <= LDS_MISC && LDS_TAB + 2048 <= 163840 && NSLOT * S2 <= LDS_MISC, "LDS map");
#define G0END 0
#define PRIO 1
#define NE1 9
#define NE2 3
#define WARM_T 3
constexpr unsigned SPIN_CAP = 1u << 16;

struct Params {
    const u16 *x, *lre, *lim, *bre, *bim, *cre, *cim, *logdt, *dsk, *wg, *wu, *wd, *gw;
    float* out;
    char* ws;
    unsigned job[256];
};

#define WAITV(n) asm volatile("s_waitcnt vmcnt(" #n ")" ::: "memory")
#define WAITL0() asm volatile("s_waitcnt lgkmcnt(0)" ::: "memory")
#define BAR() do { asm volatile("" ::: "memory"); __builtin_amdgcn_s_barrier(); asm volatile("" ::: "memory"); } while (0)
#define SCHED() __builtin_amdgcn_sched_barrier(0)

#define CS(i) do {} while (0)
#define STAMP(k) do {} while (0)

DI void glds16(unsigned voff, const void* sbase, unsigned ldsdst) {
    asm volatile("s_mov_b32 m0, %2\n\ts_nop 0\n\tglobal_load_lds_dwordx4 %0, %1" ::"v"(voff), "s"(sbase), "s"(ldsdst) : "memory");
}
DI u32x4 gload_sc1(unsigned voff, const void* sbase) {
    u32x4 r;
    asm volatile("global_load_dwordx4 %0, %1, %2 sc1" : "=v"(r) : "v"(voff), "s"(sbase) : "memory");
    return r;
}
DI float bf2f(unsigned v) { return __uint_as_float(v << 16); }
DI float bflo(unsigned v) { return __uint_as_float(v << 16); }
DI float bfhi(unsigned v) { return __uint_as_float(v & 0xffff0000u); }
DI unsigned pack2(float a, float b) { bf16x2 v = {(__bf16)a, (__bf16)b}; return __builtin_bit_cast(unsigned, v); }
DI float rbf(float a) { return bflo(pack2(a, a)); }
DI bf16x8 ldsr128(unsigned a) { return *(const LAS bf16x8*)(size_t)a; }
DI bf16x8 ldstr(unsigned a0, unsigned a1) {
    s16x4 lo = __builtin_amdgcn_ds_read_tr16_b64_v4i16((LAS s16x4*)(size_t)a0);
    s16x4 hi = __builtin_amdgcn_ds_read_tr16_b64_v4i16((LAS s16x4*)(size_t)a1);
    s16x8 v = __builtin_shufflevector(lo, hi, 0, 1, 2, 3, 4, 5, 6, 7);
    return __builtin_bit_cast(bf16x8, v);
}
template <int N> struct IC { static constexpr int value = N; };
#define MFMA(a, b, c) __builtin_amdgcn_mfma_f32_16x16x32_bf16((a), (b), (c), 0, 0, 0)
#define RLX __ATOMIC_RELAXED, __HIP_MEMORY_SCOPE_AGENT

DI float vfma(float a, float b, float c) { return __builtin_fmaf(a, b, c); }
DI float vmul(float a, float b) { return a * b; }
DI void vfmac(float& c, float a, float b) { c = __builtin_fmaf(a, b, c); }
DI void horner2(float& ar, float& ai, float& br, float& bi, float lar, float lai, float nlai, float lbr, float lbi, float nlbi,
                float a0r, float a0i, float b0r, float b0i, float a1r, float a1i, float b1r, float b1i) {
    float t0, t1, t2, t3;
    asm("v_fma_f32 %4, %8, %0, %14\n\tv_fma_f32 %5, %8, %1, %15\n\tv_fma_f32 %6, %11, %2, %16\n\tv_fma_f32 %7, %11, %3, %17\n\t"
        "v_fmac_f32 %4, %10, %1\n\tv_fmac_f32 %5, %9, %0\n\tv_fmac_f32 %6, %13, %3\n\tv_fmac_f32 %7, %12, %2\n\t"
        "v_fma_f32 %0, %8, %4, %18\n\tv_fma_f32 %1, %8, %5, %19\n\tv_fma_f32 %2, %11, %6, %20\n\tv_fma_f32 %3, %11, %7, %21\n\t"
        "v_fmac_f32 %0, %10, %5\n\tv_fmac_f32 %1, %9, %4\n\tv_fmac_f32 %2, %13, %7\n\tv_fmac_f32 %3, %12, %6"
        : "+v"(ar), "+v"(ai), "+v"(br), "+v"(bi), "=&v"(t0), "=&v"(t1), "=&v"(t2), "=&v"(t3)
        : "v"(lar), "v"(lai), "v"(nlai), "v"(lbr), "v"(lbi), "v"(nlbi), "v"(a0r), "v"(a0i), "v"(b0r), "v"(b0i), "v"(a1r), "v"(a1i), "v"(b1r), "v"(b1i));
}
DI void cscale4(f32x4& re, f32x4& im, float cr, float ci, float nci) {
    float r0, r1, r2, r3, i0, i1, i2, i3;
    asm("v_mul_f32 %0, %8, %11\n\tv_mul_f32 %1, %8, %12\n\tv_mul_f32 %2, %8, %13\n\tv_mul_f32 %3, %8, %14\n\t"
        "v_mul_f32 %4, %8, %15\n\tv_mul_f32 %5, %8, %16\n\tv_mul_f32 %6, %8, %17\n\tv_mul_f32 %7, %8, %18\n\t"
        "v_fmac_f32 %0, %10, %15\n\tv_fmac_f32 %1, %10, %16\n\tv_fmac_f32 %2, %10, %17\n\tv_fmac_f32 %3, %10, %18\n\t"
        "v_fmac_f32 %4, %9, %11\n\tv_fmac_f32 %5, %9, %12\n\tv_fmac_f32 %6, %9, %13\n\tv_fmac_f32 %7, %9, %14"
        : "=&v"(r0), "=&v"(r1), "=&v"(r2), "=&v"(r3), "=&v"(i0), "=&v"(i1), "=&v"(i2), "=&v"(i3)
        : "v"(cr), "v"(ci), "v"(nci), "v"(re[0]), "v"(re[1]), "v"(re[2]), "v"(re[3]), "v"(im[0]), "v"(im[1]), "v"(im[2]), "v"(im[3]));
    re = (f32x4){r0, r1, r2, r3}; im = (f32x4){i0, i1, i2, i3};
}
DI void scan4(float& hr, float& hi, const f32x4& ur, const f32x4& ui, float lr, float li, float nli, u32x2& ore, u32x2& oim) {
    float r0 = ur[0], r1 = ur[1], r2 = ur[2], r3 = ur[3], i0 = ui[0], i1 = ui[1], i2 = ui[2], i3 = ui[3];
    unsigned a, b, c, d;
    asm("v_fmac_f32 %0, %12, %15\n\tv_fmac_f32 %4, %12, %16\n\tv_fmac_f32 %0, %14, %16\n\tv_fmac_f32 %4, %13, %15\n\t"
        "v_fmac_f32 %1, %12, %0\n\tv_fmac_f32 %5, %12, %4\n\tv_fmac_f32 %1, %14, %4\n\tv_fmac_f32 %5, %13, %0\n\t"
        "v_fmac_f32 %2, %12, %1\n\tv_fmac_f32 %6, %12, %5\n\tv_fmac_f32 %2, %14, %5\n\tv_fmac_f32 %6, %13, %1\n\t"
        "v_fmac_f32 %3, %12, %2\n\tv_fmac_f32 %7, %12, %6\n\tv_fmac_f32 %3, %14, %6\n\tv_fmac_f32 %7, %13, %2\n\t"
        "v_cvt_pk_bf16_f32 %8, %0, %1\n\tv_cvt_pk_bf16_f32 %9, %2, %3\n\tv_cvt_pk_bf16_f32 %10, -%4, -%5\n\tv_cvt_pk_bf16_f32 %11, -%6, -%7"
        : "+v"(r0), "+v"(r1), "+v"(r2), "+v"(r3), "+v"(i0), "+v"(i1), "+v"(i2), "+v"(i3), "=&v"(a), "=&v"(b), "=&v"(c), "=&v"(d)
        : "v"(lr), "v"(li), "v"(nli), "v"(hr), "v"(hi));
    hr = r3; hi = i3; ore = (u32x2){a, b}; oim = (u32x2){c, d};
}
DI unsigned gload_u16(unsigned voff, const void* sbase) { unsigned r; asm volatile("global_load_ushort %0, %1, %2" : "=v"(r) : "v"(voff), "s"(sbase) : "memory"); return r; }
DI void cmul(float ar, float ai, float br, float bi, float& cr, float& ci) { cr = ar * br - ai * bi; ci = ar * bi + ai * br; }

DI void ssm_params(unsigned raw_dt, unsigned raw_re, unsigned raw_im, float& lr, float& li, float& cr, float& ci) {
    const float dt = rbf(expf(bf2f(raw_dt)));
    const float ar = bf2f(raw_re), ai = bf2f(raw_im);
    const float e = expf(dt * ar);
    float sn, cs;
    sincosf(dt * ai, &sn, &cs);
    lr = e * cs; li = e * sn;
    const float nr = lr - 1.0f, ni = li, den = ar * ar + ai * ai;
    cr = 2.0f * ((nr * ar + ni * ai) / den);
    ci = 2.0f * ((ni * ar - nr * ai) / den);
}
DI void pow20(float lr, float li, float& r, float& i) {
    float a2r, a2i, a4r, a4i, a5r, a5i, a10r, a10i;
    cmul(lr, li, lr, li, a2r, a2i); cmul(a2r, a2i, a2r, a2i, a4r, a4i); cmul(a4r, a4i, lr, li, a5r, a5i);
    cmul(a5r, a5i, a5r, a5i, a10r, a10i); cmul(a10r, a10i, a10r, a10i, r, i);
}

#define CSARG
#define CSPASS
DI void mfma_acc(f32x4& c, const bf16x8& a, const bf16x8& b) { asm volatile("v_mfma_f32_16x16x32_bf16 %0, %1, %2, %0" : "+v"(c) : "v"(a), "v"(b)); }
DI void set_m0(unsigned a, unsigned b) { asm volatile("s_add_i32 m0, %0, %1" ::"s"(a), "s"(b) : "memory", "scc"); }
template <int N> DI void set_m0i(unsigned a) { asm volatile("s_add_i32 m0, %0, %1" ::"s"(a), "n"(N) : "memory", "scc"); }
DI void glds_m0(unsigned voff, const void* sbase) {
    asm volatile("global_load_lds_dwordx4 %0, %1" ::"v"(voff), "s"(sbase) : "memory"); }
DI void glds_m0_half(unsigned voff, const void* sbase) {
    asm volatile("s_mov_b32 exec_hi, 0\n\tglobal_load_lds_dwordx4 %0, %1\n\ts_mov_b32 exec_hi, -1" ::"v"(voff), "s"(sbase) : "memory");
}
template <int N> DI void waitv0() { asm volatile("s_waitcnt vmcnt(%0)" ::"n"(N) : "memory"); }
template <int N> DI unsigned smul(unsigned a) { unsigned r; asm("s_mul_i32 %0, %1, %2" : "=s"(r) : "s"(a), "n"(N)); return r; }
#define LGKM0() __builtin_amdgcn_s_waitcnt(0xC07F)
DI unsigned opaque(unsigned v) { asm volatile("" : "+v"(v)); return v; }

template <bool SSM, int TH, class H0, class H, class H2>
DI void p1_main(const Params& p, f32x4 (&acc)[5][4], int pan, int ct, int w, int lane, unsigned lds0, H0 gap, H pre, H2 mid CSARG) {
    constexpr int th = TH;
    const int c = w & 3, t0 = pan * PM;
    constexpr int NX = TH ? 2 : 3, NP = NX + 4;
    unsigned xoff[3], woff[4];
#pragma unroll
    for (int i = 0; i < NX; ++i) {
        const int row = 8 * (w + 8 * i) + (lane >> 3);
        int tr = row;
        if (SSM) { const int hh = row >= 80 ? 1 : 0, rr = row - 80 * hh, b = rr >> 4, rho = rr & 15; tr = 80 * hh + 20 * (rho >> 2) + 4 * b + (rho & 3); }
        const int g = min(t0 + tr, L - 1);
        xoff[i] = (unsigned)g * 1280u + (unsigned)(((lane & 7) ^ ((row >> 1) & 7)) * 16);
    }
    const char* wb0; const char* wb1; unsigned wstep;
    if (!SSM) {
        const int kq = lane >> 4, swz = kq | (((w >> 2) & 1) << 2);
        int j = (lane & 15) ^ (swz << 1);
        if (ct == 13) j &= 7;
#pragma unroll
        for (int i = 0; i < 4; ++i) woff[i] = (unsigned)(4 * (w >> 1) + 16 * i + kq) * 3456u + (unsigned)(128 * ct + 8 * j) * 2u;
        wb0 = wb1 = (const char*)((w & 1) ? p.wu : p.wg);
        wstep = 64u * 3456u;
    } else {
        const int r = 8 * w + (lane >> 3);
#pragma unroll
        for (int i = 0; i < 4; ++i) woff[i] = (unsigned)(128 * ct + r + 64 * (i & 1)) * 1280u + (unsigned)(((lane & 7) ^ ((r >> 1) & 7)) * 16);
        wb0 = (const char*)p.bre; wb1 = (const char*)p.bim;
        wstep = 128u;
    }
    const char* xb = (const char*)p.x;
    const unsigned mX = lds0 + w * 1024, mW = lds0 + XS + w * 1024;
    auto stW = [&](unsigned slot, int i) { set_m0(slot, mW + i * 8192); asm volatile("s_nop 0"); glds_m0(woff[i], (SSM && i >= 2) ? wb1 : wb0); };
    auto stX = [&](unsigned slot, int i) { set_m0(slot, mX + i * 8192); asm volatile("s_nop 0"); glds_m0(xoff[i], xb); };
    const int rho = lane & 15, q = lane >> 4;
    const unsigned xa0 = lds0 + (unsigned)(80 * th + rho) * 128u + (unsigned)((q ^ ((rho >> 1) & 7)) * 16), xa1 = xa0 ^ 64u;
    unsigned wa0, wa1;
    if (!SSM) {
        const int qq = (lane & 15) >> 2, pp = lane & 3, x = (qq << 1) | ((q & 1) << 3);
        wa0 = lds0 + XS + q * 4096 + qq * 256 + (((4 * c + (pp >> 1)) ^ x) * 16) + 8 * (pp & 1);
        wa1 = wa0 ^ 32u;
    } else {
        wa0 = lds0 + XS + (unsigned)(32 * c + rho) * 128u + (unsigned)((q ^ ((rho >> 1) & 7)) * 16);
        wa1 = wa0 ^ 64u;
    }
    bf16x8 xf[2][5], wf[2][4];
    unsigned bx, bw0, bw1;
    auto bases = [&](unsigned slot, int s) {
        bx = opaque(slot + (s ? xa1 : xa0));
        if (!SSM) { bw0 = opaque(slot + wa0); bw1 = opaque(slot + wa1); } else bw0 = opaque(slot + (s ? wa1 : wa0));
    };
    auto rdW = [&](int set, int s, int i) {
        if (!SSM) { const unsigned a = ((i >> 1) ? bw1 : bw0) + s * 16384 + (i & 1) * 1024; wf[set][i] = ldstr(a, a + 2048); }
        else wf[set][i] = ldsr128(bw0 + (i >> 1) * 16384 + (i & 1) * 2048);
    };
    auto rdX = [&](int set, int b) {
        xf[set][b] = ldsr128(bx + b * 2048); };
    auto mm = [&](int set, int i) {
        if (SSM) mfma_acc(acc[i >> 2][i & 3], xf[set][i >> 2], wf[set][i & 3]); else mfma_acc(acc[i >> 2][i & 3], wf[set][i & 3], xf[set][i >> 2]);
    };
#define KST(i) do {} while (0)
    KST(22); CS(60);
    auto stage = [&](unsigned slot) {
#pragma unroll
        for (int i = 0; i < 4; ++i) stW(slot, i);
#pragma unroll
        for (int i = 0; i < NX; ++i) stX(slot, i);
        wb0 += wstep; wb1 += wstep; xb += 128;
    };
    stage(0);
    SCHED();
    gap();
#pragma unroll
    for (int b = 0; b < 5; ++b)
#pragma unroll
        for (int blk = 0; blk < 4; ++blk) acc[b][blk] = (f32x4){0.f, 0.f, 0.f, 0.f};
    asm volatile("" : "+v"(acc[0][0]), "+v"(acc[0][1]), "+v"(acc[0][2]), "+v"(acc[0][3]), "+v"(acc[1][0]), "+v"(acc[1][1]), "+v"(acc[1][2]), "+v"(acc[1][3]),
                      "+v"(acc[2][0]), "+v"(acc[2][1]), "+v"(acc[2][2]), "+v"(acc[2][3]), "+v"(acc[3][0]), "+v"(acc[3][1]), "+v"(acc[3][2]), "+v"(acc[3][3]),
                      "+v"(acc[4][0]), "+v"(acc[4][1]), "+v"(acc[4][2]), "+v"(acc[4][3]));
    SCHED();
    stage(STG);
    CS(61);
    waitv0<2 * NP>();
    pre();
    waitv0<NP>(); CS(62);
    BAR();
    if (TH == 1) __builtin_amdgcn_s_setprio(1);
    bases(0, 0);
#pragma unroll
    for (int i = 0; i < 4; ++i) rdW(0, 0, i);
#pragma unroll
    for (int i = 0; i < 5; ++i) rdX(0, i);
    bases(0, 1);
    if (TH == 1) stage(2 * STG);
    KST(0); CS(63);
    unsigned cur = 0, nx1 = STG, nx2 = 2 * STG;
    auto body = [&](auto kind, int t) {
        constexpr int K = decltype(kind)::value;
        LGKM0(); SCHED();
#pragma unroll
        for (int i = 0; i < 20; ++i) {
            mm(0, i);
            if (i < 4) rdW(1, 1, i);
            else if (i < 9) rdX(1, i - 4);
            if (TH == 0 && K < 2 && i >= 5 && i < 19) {
                const int k = (i - 5) >> 1;
                if (((i - 5) & 1) == 0) set_m0(nx2, k < 4 ? mW + k * 8192 : mX + (k - 4) * 8192);
                else if (k < 4) glds_m0(woff[k], (SSM && k >= 2) ? wb1 : wb0);
                else glds_m0(xoff[k - 4], xb);
            }
            if (K < 3 && i == 19) bases(nx1, 0);
            SCHED();
        }
        if (TH == 0 && K < 2) { wb0 += wstep; wb1 += wstep; xb += 128; }
        if (K == 1) mid();
        if (K == 0) waitv0<NP>(); else if (K == 1) waitv0<NP + 1>(); else if (K == 2) waitv0<1>();
        KST(2 * t + 1);
        LGKM0(); BAR(); SCHED();
        KST(2 * t + 2);
        if (TH == 1 && K == 0) { stage(cur); SCHED(); }
#pragma unroll
        for (int i = 0; i < 20; ++i) {
            mm(1, i);
            if (K < 3) {
                if (i < 4) rdW(0, 0, i);
                else if (i < 9) rdX(0, i - 4);
                else if (i == 18) bases(nx1, 1);
            }
            SCHED();
        }
        const unsigned o = cur; cur = nx1; nx1 = nx2; nx2 = o;
    };
#pragma unroll 1
    for (int t = 0; t < 7; ++t) body(IC<0>{}, t);
    body(IC<1>{}, 7); body(IC<2>{}, 8); body(IC<3>{}, 9);
    if (TH == 1) __builtin_amdgcn_s_setprio(0);
    asm volatile("s_nop 7\n\ts_nop 7"
                 : "+v"(acc[0][0]), "+v"(acc[0][1]), "+v"(acc[0][2]), "+v"(acc[0][3]), "+v"(acc[1][0]), "+v"(acc[1][1]), "+v"(acc[1][2]), "+v"(acc[1][3]),
                   "+v"(acc[2][0]), "+v"(acc[2][1]), "+v"(acc[2][2]), "+v"(acc[2][3]), "+v"(acc[3][0]), "+v"(acc[3][1]), "+v"(acc[3][2]), "+v"(acc[3][3]),
                   "+v"(acc[4][0]), "+v"(acc[4][1]), "+v"(acc[4][2]), "+v"(acc[4][3])::"memory");
    KST(21);
}

template <int N> DI void waitv() { asm volatile("s_waitcnt vmcnt(%0)" ::"n"(N) : "memory"); }
constexpr int p2_younger(int NT, int T, int e) {
    int n = 0; bool seen = false;
    for (int t = 0; t < 3; ++t)
        for (int h = 0; h < 2; ++h) { if (seen) n += 2; if (t == T && h == e) seen = true; }
    for (int t = 0; t < NT; ++t) {
        if (t == T && e == 0) return n;
        if (t + 3 < NT) { if (seen) n += 2; if (t + 3 == T && e == 0) seen = true; }
        if (t == T && e == 1) return n;
        if (t + (int)NSLOT < NT && seen) n += 3;
        if (t + 3 < NT) { if (seen) n += 2; if (t + 3 == T && e == 1) seen = true; }
    }
    return n;
}
struct W2Src { unsigned vo[3]; };
template <bool DOWN> DI W2Src w2_src(int tj, int w, int lane, unsigned lds0) {
    W2Src r;
#pragma unroll
    for (int i = 0; i < 3; ++i) {
        if (DOWN) { const int idx = min(160 * w + 64 * i + lane, 1279), row = idx / 10, gr = idx - 10 * row; r.vo[i] = (unsigned)row * 1280u + (unsigned)(80 * tj + 8 * gr) * 2u; }
        else {
            const int row = min(20 * w + 8 * i + (lane >> 3), 159), hi = row >= 80 ? 1 : 0;
            r.vo[i] = (unsigned)(80 * tj + row - 80 * hi) * 1280u + (unsigned)(128 * hi) + (unsigned)(((lane & 7) ^ ((row >> 1) & 7)) * 16);
        }
    }
    return r;
}
template <bool DOWN> DI const char* w2_base(const Params& p, int T, int w) {
    if (DOWN) return (const char*)p.wd + (unsigned)T * (128u * 1280u) - ((T == 13 && w >= 4) ? 64u * 1280u : 0u);
    return (const char*)(T >= 5 ? p.cim : p.cre) + (unsigned)(T >= 5 ? T - 5 : T) * 256u;
}
template <bool DOWN> DI void w2_piece(const Params& p, const W2Src& s, int T, int w, int i) {
    set_m0(smul<2560>(w), (unsigned)((T % NSLOT) * S2 + i * 1024));
    asm volatile("s_nop 0");
    if (i < 2) glds_m0(s.vo[i], w2_base<DOWN>(p, T, w)); else glds_m0_half(s.vo[i], w2_base<DOWN>(p, T, w));
}
DI const char* l2_warm_addr(const Params& p, int b, int w, int lane) {
    const char *wd = (const char*)p.wd, *cre = (const char*)p.cre, *cim = (const char*)p.cim;
    asm volatile("" : "+v"(lane), "+s"(wd), "+s"(cre), "+s"(cim));
    const int cg = b & 1, l = ((b >> 3) * 8 + w) * 64 + lane;
    if (l < 8640) { const int row = l / 5; return wd + row * 1280 + 640 * cg + 128 * (l - 5 * row); }
    const int m = min(l - 8640, 6399), im = m >= 3200 ? 1 : 0, r = m - 3200 * im, row = r / 10;
    return (im ? cim : cre) + (320 * cg + row) * 1280 + 128 * (r - 10 * row);
}
DI void l2_warm(const char* a) {
    asm volatile("s_mov_b32 m0, %1\n\ts_nop 0\n\tglobal_load_lds_dword %0, off" ::"v"(a), "s"(LDS_JUNK) : "memory");
}
DI void tie2(u32x4& a, u32x4& b) { asm volatile("" : "+v"(a), "+v"(b)); }

template <bool DOWN> DI void p2_loop(const Params& p, f32x4 (&acc)[2][5], const W2Src& src, int ti, int w, int lane, unsigned lds0 CSARG) {
    constexpr int NT = DOWN ? 14 : 10;
    const int tg = w & 3, kh = w >> 2;
    const char* a2 = p.ws + WS_A2 + (unsigned)((DOWN ? 0 : KSF) + 2 * kh) * 1024u;
    unsigned va[2];
#pragma unroll
    for (int m = 0; m < 2; ++m) va[m] = (unsigned)((8 * ti + 2 * tg + (m ^ kh)) * KS2) * 1024u + lane * 16;
    u32x4 A[3][2][2];
    auto ldA = [&](int T, int e) {
#pragma unroll
        for (int m = 0; m < 2; ++m) A[T % 3][e][m] = gload_sc1(va[m], a2 + (4 * T + e) * 1024);
    };
    ldA(0, 0); ldA(0, 1);
    unsigned lb0, lb1;
    if (DOWN) { const int g = lane >> 4, qq = (lane & 15) >> 2, pp = lane & 3; lb0 = lds0 + (unsigned)(64 * kh + 4 * g + qq) * 160u + 8u * pp; lb1 = lb0 + 5120; }
    else { const int rho = lane & 15, q = lane >> 4; lb0 = lds0 + (unsigned)(80 * kh + rho) * 128u + (unsigned)((q ^ ((rho >> 1) & 7)) * 16); lb1 = lb0 ^ 64u; }
    bf16x8 wf[2][5];
    unsigned rb;
    auto rdW = [&](int set, int n) { if (DOWN) wf[set][n] = ldstr(rb + n * 32, rb + n * 32 + 2560); else wf[set][n] = ldsr128(rb + n * 2048); };
#pragma unroll
    for (int m = 0; m < 2; ++m)
#pragma unroll
        for (int n = 0; n < 5; ++n) acc[m][n] = (f32x4){0.f, 0.f, 0.f, 0.f};
    asm volatile("" : "+v"(acc[0][0]), "+v"(acc[0][1]), "+v"(acc[0][2]), "+v"(acc[0][3]), "+v"(acc[0][4]), "+v"(acc[1][0]), "+v"(acc[1][1]), "+v"(acc[1][2]),
                      "+v"(acc[1][3]), "+v"(acc[1][4]));
    waitv<4>(); BAR();
    ldA(1, 0); ldA(1, 1); ldA(2, 0); ldA(2, 1);
    rb = opaque(lb0);
#pragma unroll
    for (int n = 0; n < 5; ++n) rdW(0, n);
#define DBGNAN(v) do {} while (0)
    auto step = [&](auto tc) {
        constexpr int T = decltype(tc)::value;
        const bool live = !(DOWN && T == 13) || kh == 0;
        waitv<p2_younger(NT, T, 0)>(); tie2(A[T % 3][0][0], A[T % 3][0][1]);
        rb = opaque(lb1 + (T % NSLOT) * S2);
        LGKM0(); SCHED();
        if (live) {
#pragma unroll
            for (int i = 0; i < 10; ++i) {
                mfma_acc(acc[i / 5][i % 5], wf[0][i % 5], __builtin_bit_cast(bf16x8, A[T % 3][0][i / 5]));
                if (i < 5) rdW(1, i);
                SCHED();
            }
        }
        DBGNAN(2 * T + 1);
        if (T + 3 < NT) ldA(T + 3, 0);
        waitv<p2_younger(NT, T, 1)>(); tie2(A[T % 3][1][0], A[T % 3][1][1]);
        LGKM0(); BAR(); SCHED();
        if (T + 1 < NT) rb = opaque(lb0 + ((T + 1) % NSLOT) * S2);
        if (live) {
#pragma unroll
            for (int i = 0; i < 10; ++i) {
                mfma_acc(acc[i / 5][i % 5], wf[1][i % 5], __builtin_bit_cast(bf16x8, A[T % 3][1][i / 5]));
                if (T + 1 < NT && i < 5) rdW(0, i);
                if (T + (int)NSLOT < NT && i >= 5 && i < 8) w2_piece<DOWN>(p, src, T + NSLOT, w, i - 5);
                SCHED();
            }
        }
        DBGNAN(2 * T + 2);
        if (T + 3 < NT) ldA(T + 3, 1);
        SCHED();
    };
    step(IC<0>{}); step(IC<1>{}); step(IC<2>{}); step(IC<3>{}); step(IC<4>{}); step(IC<5>{}); step(IC<6>{}); step(IC<7>{}); step(IC<8>{}); step(IC<9>{});
    if (DOWN) { step(IC<10>{}); step(IC<11>{}); step(IC<12>{}); step(IC<13>{}); }
    asm volatile("s_nop 7\n\ts_nop 7"
                 : "+v"(acc[0][0]), "+v"(acc[0][1]), "+v"(acc[0][2]), "+v"(acc[0][3]), "+v"(acc[0][4]), "+v"(acc[1][0]), "+v"(acc[1][1]), "+v"(acc[1][2]),
                   "+v"(acc[1][3]), "+v"(acc[1][4])::"memory");
    const unsigned xw = lds0 + w * 5120 + lane * 16, xr = lds0 + (w ^ 4) * 5120 + lane * 16;
#pragma unroll
    for (int n = 0; n < 5; ++n) *(LAS f32x4*)(size_t)(xw + n * 1024) = acc[1][n];
    WAITL0(); BAR();
#pragma unroll
    for (int n = 0; n < 5; ++n) acc[0][n] += *(const LAS f32x4*)(size_t)(xr + n * 1024);
}
static_assert(p2_younger(10, 0, 0) == 10 && p2_younger(10, 0, 1) == 10 && p2_younger(14, 3, 0) == 19 && p2_younger(14, 3, 1) == 16 && p2_younger(10, 9, 1) == 0, "vmcnt schedule");

DI unsigned long long ld_flag(const char* ws, unsigned off) { return __hip_atomic_load((GAS unsigned long long*)(ws + off), RLX); }

DI void spin_flags(const char* ws, unsigned off, unsigned long long tag) {
    for (unsigned spins = 0;; ++spins) {
        if (__all(ld_flag(ws, off) == tag) || spins > SPIN_CAP) break;
        __builtin_amdgcn_s_sleep(1);
    }
    asm volatile("" ::: "memory");
}

__global__ __launch_bounds__(512, 2) void s5_swiglu_mix_kernel(Params p) {
    __shared__ __attribute__((aligned(1024))) char lds[LDS_BYTES];
    const int tid = threadIdx.x, lane = tid & 63;
    const int w = __builtin_amdgcn_readfirstlane(tid >> 6);
    const int b = blockIdx.x;
    const unsigned lds0 = (unsigned)(size_t)(LAS char*)lds;
    CS(0);
    STAMP(0);
    const unsigned job = p.job[b];
    u32x2 e_in;
    asm volatile("global_load_dwordx2 %0, %1, %2 sc1" : "=v"(e_in) : "v"(0u), "s"(p.ws + WS_EPOCH) : "memory");
    unsigned tag0 = 0, tag1 = 0;
    unsigned long long tag = 0;
    auto take_tag = [&]() { asm volatile("" : "+v"(e_in)); tag0 = e_in[0] + 1u; tag1 = e_in[1] + 1u; tag = ((unsigned long long)tag1 << 32) | tag0; };
    const __amdgpu_buffer_rsrc_t a2r = __builtin_amdgcn_make_buffer_rsrc(p.ws + WS_A2, 0, A2_BYTES, 0x00020000);

    const int role = job & 3;
    const int pan = (job >> 2) & 15, ct = (job >> 6) & 15;
    const bool down = (job >> 10) & 1;
    const int pair = job >> 11, ti = pair >> 3, tj = pair & 7;
    const unsigned flag_off = WS_FLAG + (role == 0 ? pan * 32 + ct : role == 1 ? pan * 32 + 16 + ct : role == 2 ? 13 * 32 + pan : 13 * 32 + 8) * 8;
    W2Src src;
    auto make_src = [&]() { int l2 = lane; asm volatile("" : "+v"(l2)); src = down ? w2_src<true>(tj, w, l2, lds0) : w2_src<false>(tj, w, l2, lds0); };

    const char* warm_a;
    auto setup = [&]() { make_src(); warm_a = l2_warm_addr(p, b, w, lane); };
    auto warm = [&]() { l2_warm(warm_a); };

    if (role == 0) {
        f32x4 acc[5][4];
        if (w < 4) p1_main<false, 0>(p, acc, pan, ct, w, lane, lds0, setup, take_tag, warm CSPASS);
        else p1_main<false, 1>(p, acc, pan, ct, w, lane, lds0, setup, take_tag, warm CSPASS);
        STAMP(1); CS(1);
        const int th = w >> 2, c = w & 3, s2 = 4 * ct + c;
        auto epi = [&](auto dn) {
            constexpr bool DN = decltype(dn)::value != 0;
            auto w2_pc = [&](int k) { w2_piece<DN>(p, src, k / 3, w, k % 3); };
#pragma unroll
            for (int bb = 0; bb < 5; ++bb) {
                float a[8];
#pragma unroll
                for (int k = 0; k < 8; ++k) a[k] = __builtin_amdgcn_exp2f(acc[bb][2 * (k >> 2)][k & 3] * -1.44269504088896f);
                if (2 * bb < NE1) w2_pc(2 * bb);
                SCHED();
#pragma unroll
                for (int k = 0; k < 8; ++k) a[k] = __builtin_amdgcn_rcpf(1.0f + a[k]);
                if (2 * bb + 1 < NE1) w2_pc(2 * bb + 1);
                SCHED();
#pragma unroll
                for (int k = 0; k < 8; ++k) a[k] = acc[bb][2 * (k >> 2)][k & 3] * a[k] * acc[bb][2 * (k >> 2) + 1][k & 3];
                const u32x4 v = {pack2(a[0], a[1]), pack2(a[2], a[3]), pack2(a[4], a[5]), pack2(a[6], a[7])};
                const int tb = 10 * pan + 5 * th + bb;
                if (tb < 128 && s2 < KSF) __builtin_amdgcn_raw_buffer_store_b128(v, a2r, (unsigned)(tb * KS2 + s2) * 1024u + lane * 16, 0, 16);
                SCHED();
            }
            STAMP(2); CS(2);
#pragma unroll
            for (int k = NE1; k < NE1 + NE2; ++k) w2_pc(k);
            waitv<NE2>(); CS(3); BAR(); CS(4);
            if (w == 0) __hip_atomic_store((GAS unsigned long long*)(p.ws + flag_off), tag, RLX);
#pragma unroll
            for (int k = NE1 + NE2; k < 3 * (int)NSLOT; ++k) w2_pc(k);
        };
        if (down) epi(IC<1>{}); else epi(IC<0>{});
        STAMP(3);
    } else if (role == 1) {
        const int th = w >> 2, c = w & 3, sg = lane & 15, q = lane >> 4;
        float t20r = 0.f, t20i = 0.f, t160r = 0.f, t160i = 0.f;
        unsigned raw[3] = {0u, 0u, 0u};
        if (w < 2) { const unsigned st = (unsigned)(128 * ct + tid) * 2u; raw[0] = gload_u16(st, p.logdt); raw[1] = gload_u16(st, p.lre); raw[2] = gload_u16(st, p.lim); }
        f32x4 acc[5][4];
        auto params = [&]() {
            take_tag();
            if (w < 2) {
                asm volatile("" : "+v"(raw[0]), "+v"(raw[1]), "+v"(raw[2]));
                float a, bq, cc, dd, xr, xi, yr, yi;
                ssm_params(raw[0], raw[1], raw[2], a, bq, cc, dd);
                *(LAS f32x4*)(size_t)(lds0 + LDS_TAB + tid * 16) = (f32x4){a, bq, cc, dd};
                pow20(a, bq, t20r, t20i);
                cmul(t20r, t20i, t20r, t20i, xr, xi); cmul(xr, xi, xr, xi, yr, yi); cmul(yr, yi, yr, yi, t160r, t160i);
                WAITL0();
            }
        };
        if (w < 4) p1_main<true, 0>(p, acc, pan, ct, w, lane, lds0, setup, params, warm CSPASS);
        else p1_main<true, 1>(p, acc, pan, ct, w, lane, lds0, setup, take_tag, warm CSPASS);
        STAMP(1); CS(1);
        float lr[2], li[2], cr[2], ci[2];
#pragma unroll
        for (int n = 0; n < 2; ++n) {
            const f32x4 t = *(const LAS f32x4*)(size_t)(lds0 + LDS_TAB + (32 * c + 16 * n + sg) * 16);
            lr[n] = t[0]; li[n] = t[1]; cr[n] = t[2]; ci[n] = t[3];
        }
        const int seg = 4 * th + q;
        const unsigned ea = lds0 + LDS_E + (seg * 128 + 32 * c + sg) * 8;
        const float nli[2] = {-li[0], -li[1]}, nci[2] = {-ci[0], -ci[1]};
        {
            float er0 = 0.f, ei0 = 0.f, er1 = 0.f, ei1 = 0.f;
#pragma unroll
            for (int i = 0; i < 20; i += 2)
                horner2(er0, ei0, er1, ei1, lr[0], li[0], nli[0], lr[1], li[1], nli[1],
                        acc[i >> 2][0][i & 3], acc[i >> 2][2][i & 3], acc[i >> 2][1][i & 3], acc[i >> 2][3][i & 3],
                        acc[i >> 2][0][(i & 3) + 1], acc[i >> 2][2][(i & 3) + 1], acc[i >> 2][1][(i & 3) + 1], acc[i >> 2][3][(i & 3) + 1]);
            *(LAS f32x2*)(size_t)ea = (f32x2){cr[0] * er0 - ci[0] * ei0, cr[0] * ei0 + ci[0] * er0};
            *(LAS f32x2*)(size_t)(ea + 128) = (f32x2){cr[1] * er1 - ci[1] * ei1, cr[1] * ei1 + ci[1] * er1};
        }
        CS(5); WAITL0(); BAR(); CS(6);
        float pr[8], pi[8];
        if (w < 2) {
            float tr = 0.f, tii = 0.f;
#pragma unroll
            for (int j = 0; j < 8; ++j) {
                const f32x2 e = *(const LAS f32x2*)(size_t)(lds0 + LDS_E + (j * 128 + tid) * 8);
                pr[j] = tr; pi[j] = tii;
                const float t = t20r * tr - t20i * tii + e[0];
                tii = t20r * tii + t20i * tr + e[1]; tr = t;
            }
            if (pan < NPAN - 1) {
                const u32x4 gv = {__float_as_uint(tr), tag0, __float_as_uint(tii), tag1};
                const __amdgpu_buffer_rsrc_t gr = __builtin_amdgcn_make_buffer_rsrc(p.ws + WS_GRAN, 0, 12 * 640 * 16, 0x00020000);
                __builtin_amdgcn_raw_buffer_store_b128(gv, gr, (unsigned)(pan * 640 + 128 * ct + tid) * 16u, 0, 16);
            }
        }
        CS(7);
#pragma unroll
        for (int k = 0; k < 10; ++k) {
            const int n = k & 1, bb = k >> 1;
            cscale4(acc[bb][n], acc[bb][2 + n], cr[n], ci[n], nci[n]);
            if (k < 9 && w >= 2) w2_piece<false>(p, src, k / 3, w, k % 3);
            SCHED();
        }
        STAMP(4); CS(8);
        if (w < 2) {
            float hr = 0.f, hi = 0.f;
            if (pan > 0) {
                const __amdgpu_buffer_rsrc_t gr = __builtin_amdgcn_make_buffer_rsrc(p.ws + WS_GRAN, 0, 12 * 640 * 16, 0x00020000);
                u32x4 gv[12];
                for (unsigned spins = 0;; ++spins) {
                    asm volatile("" ::: "memory");
                    bool ok = true;
#pragma unroll
                    for (int j = 0; j < 12; ++j) {
                        const int jj = j < pan ? j : pan - 1;
                        gv[j] = __builtin_amdgcn_raw_buffer_load_b128(gr, (unsigned)(jj * 640 + 128 * ct + tid) * 16u, 0, 16);
                    }
#pragma unroll
                    for (int j = 0; j < 12; ++j) ok &= (gv[j][1] == tag0) & (gv[j][3] == tag1);
                    if (__all(ok) || spins > SPIN_CAP) break;
                    __builtin_amdgcn_s_sleep(1);
                }
#pragma unroll
                for (int j = 0; j < 12; ++j) {
                    if (j < pan) {
                        const float t = t160r * hr - t160i * hi + __uint_as_float(gv[j][0]);
                        hi = t160r * hi + t160i * hr + __uint_as_float(gv[j][2]); hr = t;
                    }
                }
            }
#pragma unroll
            for (int j = 0; j < 8; ++j) {
                *(LAS f32x2*)(size_t)(lds0 + LDS_E + (j * 128 + tid) * 8) = (f32x2){pr[j] + hr, pi[j] + hi};
                const float t = t20r * hr - t20i * hi;
                hi = t20r * hi + t20i * hr; hr = t;
            }
#pragma unroll
            for (int k = 0; k < 9; ++k) w2_piece<false>(p, src, k / 3, w, k % 3);
        }
        CS(9); WAITL0(); BAR();
        STAMP(5); CS(10);
        const unsigned trb = lds0 + LDS_TR + w * TR_WAVE;
        {
            f32x2 cw[2];
#pragma unroll
            for (int n = 0; n < 2; ++n) cw[n] = *(const LAS f32x2*)(size_t)(ea + n * 128);
            float hr[2] = {cw[0][0], cw[1][0]}, hi[2] = {cw[0][1], cw[1][1]};
            const unsigned ta = trb + sg * TR_STRIDE + 40 * q;
#pragma unroll
            for (int bb = 0; bb < 5; ++bb) {
#pragma unroll
                for (int n = 0; n < 2; ++n) {
                    u32x2 ore, oim;
                    scan4(hr[n], hi[n], acc[bb][n], acc[bb][2 + n], lr[n], li[n], nli[n], ore, oim);
                    const unsigned a = ta + 16 * n * TR_STRIDE + 8 * bb;
                    *(LAS u32x2*)(size_t)a = ore;
                    *(LAS u32x2*)(size_t)(a + 32 * TR_STRIDE) = oim;
                }
            }
        }
        WAITL0(); CS(11);
        {
            const int qq = (lane & 15) >> 2, pp = lane & 3;
            const unsigned rb = trb + (8 * q + qq) * TR_STRIDE + 8 * pp;
#pragma unroll
            for (int comp = 0; comp < 2; ++comp)
#pragma unroll
                for (int m = 0; m < 5; ++m) {
                    const unsigned a = rb + comp * 32 * TR_STRIDE + m * 32;
                    const bf16x8 v = ldstr(a, a + 4 * TR_STRIDE);
                    const int tb = 10 * pan + 5 * th + m, s2 = KSF + comp * 20 + 4 * ct + c;
                    if (tb < 128) __builtin_amdgcn_raw_buffer_store_b128(__builtin_bit_cast(u32x4, v), a2r, (unsigned)(tb * KS2 + s2) * 1024u + lane * 16, 0, 16);
                }
        }
        STAMP(2); CS(2);
        WAITV(0); CS(3); WAITL0(); BAR(); CS(4);
        if (w == 0) __hip_atomic_store((GAS unsigned long long*)(p.ws + flag_off), tag, RLX);
#pragma unroll
        for (int k = 9; k < 3 * (int)NSLOT; ++k) w2_piece<false>(p, src, k / 3, w, k % 3);
        STAMP(3);
    } else {
        setup(); warm();
        WAITV(0); take_tag();
        if (role == 2) {
            const int i16 = lane & 15, rr = lane >> 4;
            u32x4 gw[5];
#pragma unroll
            for (int k = 0; k < 5; ++k) gw[k] = *(const u32x4*)(p.gw + 8 * (i16 + 16 * k));
            float mine = 0.f;
#pragma unroll
            for (int hf = 0; hf < 2; ++hf) {
                u32x4 xv[4][5];
#pragma unroll
                for (int ps = 0; ps < 4; ++ps)
#pragma unroll
                    for (int k = 0; k < 5; ++k) xv[ps][k] = *(const u32x4*)(p.x + (256 * pan + 32 * w + 16 * hf + 4 * ps + rr) * D + 8 * (i16 + 16 * k));
#pragma unroll
                for (int ps = 0; ps < 4; ++ps) {
                    float s = 0.f;
#pragma unroll
                    for (int k = 0; k < 5; ++k)
#pragma unroll
                        for (int e = 0; e < 4; ++e) {
                            const unsigned pk = pack2(bflo(xv[ps][k][e]) * bflo(gw[k][e]), bfhi(xv[ps][k][e]) * bfhi(gw[k][e]));
                            s += bflo(pk); s += bfhi(pk);
                        }
                    s += __shfl_xor(s, 8); s += __shfl_xor(s, 4); s += __shfl_xor(s, 2); s += __shfl_xor(s, 1);
                    const float z = rbf(s), g = rbf(1.0f / (1.0f + expf(-z)));
                    const float gsel = __shfl(g, (lane & 3) * 16);
                    if ((lane >> 2) == 4 * hf + ps) mine = gsel;
                }
            }
            if (lane < 32) __hip_atomic_store((GAS float*)(p.ws + WS_GATE) + 256 * pan + 32 * w + lane, mine, RLX);
        }
        WAITV(0); WAITL0(); BAR();
        if (w == 0) __hip_atomic_store((GAS unsigned long long*)(p.ws + flag_off), tag, RLX);
        if (down) {
#pragma unroll
            for (int k = 0; k < 3 * (int)NSLOT; ++k) w2_piece<true>(p, src, k / 3, w, k % 3);
        } else {
#pragma unroll
            for (int k = 0; k < 3 * (int)NSLOT; ++k) w2_piece<false>(p, src, k / 3, w, k % 3);
        }
        STAMP(3);
    }
    CS(12);
    if (w == 0) {
        const int p0 = (128 * ti) / PM, p1 = (128 * ti + 127) / PM, k = lane & 15, n = down ? 14 : 5;
        spin_flags(p.ws, WS_FLAG + (((lane >> 4) & 1 ? p1 : p0) * 32 + (down ? 0 : 16) + (k < n ? k : 0)) * 8, tag);
    }
    BAR();
    STAMP(6); CS(13);
    f32x4 acc2[2][5];
    if (down) p2_loop<true>(p, acc2, src, ti, w, lane, lds0 CSPASS); else p2_loop<false>(p, acc2, src, ti, w, lane, lds0 CSPASS);
    STAMP(7); CS(14);
    const int tb = 8 * ti + 2 * (w & 3) + (w >> 2), t = 16 * tb + (lane & 15), q = lane >> 4;
    const __amdgpu_buffer_rsrc_t vr = __builtin_amdgcn_make_buffer_rsrc(p.ws + WS_V, 0, V_BYTES, 0x00020000);
    const unsigned voff = (unsigned)(pair * 8 + (tb & 7)) * 3072u + lane * 16;
    spin_flags(p.ws, WS_FLAG + (13 * 32 + (t >> 8)) * 8, tag);
    const float g = __hip_atomic_load((GAS float*)(p.ws + WS_GATE) + t, RLX);
    if (down) {
        const float omg = rbf(1.0f - g);
        unsigned pk[12];
#pragma unroll
        for (int n = 0; n < 5; ++n) {
            pk[2 * n] = pack2(omg * rbf(acc2[0][n][0]), omg * rbf(acc2[0][n][1]));
            pk[2 * n + 1] = pack2(omg * rbf(acc2[0][n][2]), omg * rbf(acc2[0][n][3]));
        }
        pk[10] = pk[11] = 0u;
#pragma unroll
        for (int k = 0; k < 3; ++k) __builtin_amdgcn_raw_buffer_store_b128((u32x4){pk[4 * k], pk[4 * k + 1], pk[4 * k + 2], pk[4 * k + 3]}, vr, voff + k * 1024, 0, 16);
        WAITV(0); BAR();
        if (w == 0) __hip_atomic_store((GAS unsigned long long*)(p.ws + WS_VFLAG + pair * 8), tag, RLX);
    } else {
        const int d0 = 80 * tj + 4 * q;
        u32x2 xv[5], dv[5];
#pragma unroll
        for (int n = 0; n < 5; ++n) { xv[n] = *(const u32x2*)(p.x + t * D + d0 + 16 * n); dv[n] = *(const u32x2*)(p.dsk + d0 + 16 * n); }
        CS(15);
        if (w == 0) spin_flags(p.ws, WS_VFLAG + pair * 8, tag);
        BAR();
        CS(16);
        u32x4 vv[3];
#pragma unroll
        for (int k = 0; k < 3; ++k) vv[k] = __builtin_amdgcn_raw_buffer_load_b128(vr, voff + k * 1024, 0, 16);
#pragma unroll
        for (int n = 0; n < 5; ++n) {
            f32x4 o4;
#pragma unroll
            for (int e = 0; e < 4; ++e) {
                const unsigned xw = xv[n][e >> 1], dw = dv[n][e >> 1], vw = vv[(2 * n + (e >> 1)) >> 2][(2 * n + (e >> 1)) & 3];
                const float xd = rbf(((e & 1) ? bfhi(xw) : bflo(xw)) * ((e & 1) ? bfhi(dw) : bflo(dw)));
                o4[e] = g * (acc2[0][n][e] + xd) + ((e & 1) ? bfhi(vw) : bflo(vw));
            }
            *(f32x4*)(p.out + t * D + d0 + 16 * n) = o4;
        }
    }
    if (b == 0 && w == 7) {
        for (unsigned spins = 0;; ++spins) {
            bool ok = true;
#pragma unroll
            for (int k = 0; k < 7; ++k) {
                const int idx = 64 * k + lane, pn = idx >> 5, sl = idx & 31;
                const bool used = pn < 13 ? (sl < 14 || (sl >= 16 && sl < 21)) : sl < 9;
                ok &= ld_flag(p.ws, WS_FLAG + (used ? idx : 0) * 8) == tag;
            }
            if (__all(ok) || spins > SPIN_CAP) break;
            __builtin_amdgcn_s_sleep(1);
        }
        if (lane == 0) __hip_atomic_store((GAS unsigned long long*)(p.ws + WS_EPOCH), tag, RLX);
    }
    STAMP(8); CS(17);
}

void make_jobs(Params& p) {
    static const int cols[19] = {0, 1, 2, 3, 16, 4, 5, 6, 7, 17, 8, 9, 10, 11, 18, 12, 13, 19, 20};
    for (int x = 0; x < 8; ++x) {
        int role[32], pan[32], ct[32];
        for (int r = 0; r < 32; ++r) {
            const int n1 = 31 * x + r;
            if (r == 31) { role[r] = 2; pan[r] = x; ct[r] = 0; }
            else if (n1 >= 247) { role[r] = 3; pan[r] = 0; ct[r] = 0; }
            else {
                const int grp = n1 / 65, rem = n1 - 65 * grp, wdt = grp == 3 ? 4 : 5, pn = rem / wdt, col = cols[5 * grp + rem - pn * wdt];
                role[r] = col >= 16 ? 1 : 0; pan[r] = pn; ct[r] = col >= 16 ? col - 16 : col;
            }
        }
        int isout[32], nout = 0;
        for (int r = 0; r < 32; ++r) { isout[r] = role[r] == 1; nout += isout[r]; }
        for (int r = 0; r < 32 && nout < 16; ++r) if (role[r] == 0) { isout[r] = 1; ++nout; }
        int no = 0, nd = 0;
        for (int r = 0; r < 32; ++r) {
            const int k = isout[r] ? no++ : nd++;
            const int ti = 4 * (x >> 1) + (k >> 2), tj = 4 * (x & 1) + (k & 3), b = 8 * r + x;
            p.job[b] = (unsigned)(role[r] | (pan[r] << 2) | (ct[r] << 6) | ((isout[r] ? 0 : 1) << 10) | ((ti * 8 + tj) << 11));
        }
    }
}

}

extern "C" void kernel_launch(void* const* d_in, const int* in_sizes, int n_in, void* d_out, int out_size, void* d_ws, size_t ws_size,
                              hipStream_t stream) {
    Params p;
    p.x = (const u16*)d_in[0]; p.lre = (const u16*)d_in[1]; p.lim = (const u16*)d_in[2];
    p.bre = (const u16*)d_in[3]; p.bim = (const u16*)d_in[4]; p.cre = (const u16*)d_in[5]; p.cim = (const u16*)d_in[6];
    p.logdt = (const u16*)d_in[7]; p.dsk = (const u16*)d_in[8];
    p.wg = (const u16*)d_in[10]; p.wu = (const u16*)d_in[11]; p.wd = (const u16*)d_in[12]; p.gw = (const u16*)d_in[13];
    p.out = (float*)d_out; p.ws = (char*)d_ws;
    make_jobs(p);
    hipLaunchKernelGGL(s5_swiglu_mix_kernel, dim3(256), dim3(512), 0, stream, p);
}
```

```cpp
#include <hip/hip_runtime.h>
#include <stdint.h>

namespace {

typedef unsigned short u16;
typedef __bf16 bf16x8 __attribute__((ext_vector_type(8)));
typedef __bf16 bf16x2 __attribute__((ext_vector_type(2)));
typedef short s16x4 __attribute__((ext_vector_type(4)));
typedef short s16x8 __attribute__((ext_vector_type(8)));
typedef float f32x2 __attribute__((ext_vector_type(2)));
typedef float f32x4 __attribute__((ext_vector_type(4)));
typedef unsigned u32x2 __attribute__((ext_vector_type(2)));
typedef unsigned u32x4 __attribute__((ext_vector_type(4)));
#define LAS __attribute__((address_space(3)))
#define GAS __attribute__((address_space(1)))
#define DI __device__ __forceinline__

constexpr int L = 2048, D = 640, F = 1728;
constexpr int PM = 160, NPAN = 13;
constexpr int KS2 = 94, KSF = 54;
constexpr unsigned WS_EPOCH = 0;
constexpr unsigned WS_FLAG = 1024;
constexpr unsigned WS_GATE = 147456;
constexpr unsigned WS_GRAN = 16384;
constexpr unsigned WS_A2 = 262144;
constexpr unsigned A2_BYTES = 128u * KS2 * 1024u;
constexpr unsigned WS_V = WS_A2 + A2_BYTES;
constexpr unsigned V_BYTES = 128u * 40u * 1024u;
constexpr unsigned WS_WT = WS_V + V_BYTES;
constexpr unsigned WT_BYTES = 8u * 14u * 20480u;
constexpr unsigned WS_TFLAG = 4864;
constexpr unsigned WS_STAMP = WS_WT + WT_BYTES;
constexpr unsigned XS = 20480, STG = 53248;
constexpr unsigned LDS_MISC = 159744;
constexpr unsigned LDS_BYTES = 163840;
constexpr unsigned S2 = 20480, NSLOT = 7;
constexpr unsigned TR_STRIDE = 168, TR_WAVE = 64 * TR_STRIDE;
constexpr unsigned LDS_TR = 3 * S2, LDS_E = LDS_TR + 8 * TR_WAVE;
constexpr unsigned LDS_JUNK = LDS_MISC + 2560;
constexpr unsigned LDS_TAB = LDS_MISC + 64;
static_assert(LDS_E + 8192 <= LDS_MISC && LDS_TAB + 2048 <= 163840 && NSLOT * S2 <= LDS_MISC, "LDS map");
#define G0END 0
#define PRIO 1
#define NEARLY 7
#define NE1 6
#define NE2 6
#define WARM_T 3
constexpr unsigned SPIN_CAP = 1u << 16;

struct Params {
    const u16 *x, *lre, *lim, *bre, *bim, *cre, *cim, *logdt, *dsk, *wg, *wu, *wd, *gw;
    float* out;
    char* ws;
    unsigned job[256];
};

#define WAITV(n) asm volatile("s_waitcnt vmcnt(" #n ")" ::: "memory")
#define WAITL0() asm volatile("s_waitcnt lgkmcnt(0)" ::: "memory")
#define BAR() do { asm volatile("" ::: "memory"); __builtin_amdgcn_s_barrier(); asm volatile("" ::: "memory"); } while (0)
#define SCHED() __builtin_amdgcn_sched_barrier(0)

#define CS(i) do {} while (0)
#define STAMP(k) do {} while (0)

DI void glds16(unsigned voff, const void* sbase, unsigned ldsdst) {
    asm volatile("s_mov_b32 m0, %2\n\ts_nop 0\n\tglobal_load_lds_dwordx4 %0, %1" ::"v"(voff), "s"(sbase), "s"(ldsdst) : "memory");
}
DI u32x4 gload_sc1(unsigned voff, const void* sbase) {
    u32x4 r;
    asm volatile("global_load_dwordx4 %0, %1, %2 sc1" : "=v"(r) : "v"(voff), "s"(sbase) : "memory");
    return r;
}
DI float bf2f(unsigned v) { return __uint_as_float(v << 16); }
DI float bflo(unsigned v) { return __uint_as_float(v << 16); }
DI float bfhi(unsigned v) { return __uint_as_float(v & 0xffff0000u); }
DI unsigned pack2(float a, float b) { bf16x2 v = {(__bf16)a, (__bf16)b}; return __builtin_bit_cast(unsigned, v); }
DI float rbf(float a) { return bflo(pack2(a, a)); }
DI bf16x8 ldsr128(unsigned a) { return *(const LAS bf16x8*)(size_t)a; }
DI bf16x8 ldstr(unsigned a0, unsigned a1) {
    s16x4 lo = __builtin_amdgcn_ds_read_tr16_b64_v4i16((LAS s16x4*)(size_t)a0);
    s16x4 hi = __builtin_amdgcn_ds_read_tr16_b64_v4i16((LAS s16x4*)(size_t)a1);
    s16x8 v = __builtin_shufflevector(lo, hi, 0, 1, 2, 3, 4, 5, 6, 7);
    return __builtin_bit_cast(bf16x8, v);
}
template <int N> struct IC { static constexpr int value = N; };
#define MFMA(a, b, c) __builtin_amdgcn_mfma_f32_16x16x32_bf16((a), (b), (c), 0, 0, 0)
#define RLX __ATOMIC_RELAXED, __HIP_MEMORY_SCOPE_AGENT

DI float vfma(float a, float b, float c) { return __builtin_fmaf(a, b, c); }
DI float vmul(float a, float b) { return a * b; }
DI void vfmac(float& c, float a, float b) { c = __builtin_fmaf(a, b, c); }
DI void horner2(float& ar, float& ai, float& br, float& bi, float lar, float lai, float nlai, float lbr, float lbi, float nlbi,
                float a0r, float a0i, float b0r, float b0i, float a1r, float a1i, float b1r, float b1i) {
    float t0, t1, t2, t3;
    asm("v_fma_f32 %4, %8, %0, %14\n\tv_fma_f32 %5, %8, %1, %15\n\tv_fma_f32 %6, %11, %2, %16\n\tv_fma_f32 %7, %11, %3, %17\n\t"
        "v_fmac_f32 %4, %10, %1\n\tv_fmac_f32 %5, %9, %0\n\tv_fmac_f32 %6, %13, %3\n\tv_fmac_f32 %7, %12, %2\n\t"
        "v_fma_f32 %0, %8, %4, %18\n\tv_fma_f32 %1, %8, %5, %19\n\tv_fma_f32 %2, %11, %6, %20\n\tv_fma_f32 %3, %11, %7, %21\n\t"
        "v_fmac_f32 %0, %10, %5\n\tv_fmac_f32 %1, %9, %4\n\tv_fmac_f32 %2, %13, %7\n\tv_fmac_f32 %3, %12, %6"
        : "+v"(ar), "+v"(ai), "+v"(br), "+v"(bi), "=&v"(t0), "=&v"(t1), "=&v"(t2), "=&v"(t3)
        : "v"(lar), "v"(lai), "v"(nlai), "v"(lbr), "v"(lbi), "v"(nlbi), "v"(a0r), "v"(a0i), "v"(b0r), "v"(b0i), "v"(a1r), "v"(a1i), "v"(b1r), "v"(b1i));
}
DI void cscale4(f32x4& re, f32x4& im, float cr, float ci, float nci) {
    float r0, r1, r2, r3, i0, i1, i2, i3;
    asm("v_mul_f32 %0, %8, %11\n\tv_mul_f32 %1, %8, %12\n\tv_mul_f32 %2, %8, %13\n\tv_mul_f32 %3, %8, %14\n\t"
        "v_mul_f32 %4, %8, %15\n\tv_mul_f32 %5, %8, %16\n\tv_mul_f32 %6, %8, %17\n\tv_mul_f32 %7, %8, %18\n\t"
        "v_fmac_f32 %0, %10, %15\n\tv_fmac_f32 %1, %10, %16\n\tv_fmac_f32 %2, %10, %17\n\tv_fmac_f32 %3, %10, %18\n\t"
        "v_fmac_f32 %4, %9, %11\n\tv_fmac_f32 %5, %9, %12\n\tv_fmac_f32 %6, %9, %13\n\tv_fmac_f32 %7, %9, %14"
        : "=&v"(r0), "=&v"(r1), "=&v"(r2), "=&v"(r3), "=&v"(i0), "=&v"(i1), "=&v"(i2), "=&v"(i3)
        : "v"(cr), "v"(ci), "v"(nci), "v"(re[0]), "v"(re[1]), "v"(re[2]), "v"(re[3]), "v"(im[0]), "v"(im[1]), "v"(im[2]), "v"(im[3]));
    re = (f32x4){r0, r1, r2, r3}; im = (f32x4){i0, i1, i2, i3};
}
DI void scan4(float& hr, float& hi, const f32x4& ur, const f32x4& ui, float lr, float li, float nli, u32x2& ore, u32x2& oim) {
    float r0 = ur[0], r1 = ur[1], r2 = ur[2], r3 = ur[3], i0 = ui[0], i1 = ui[1], i2 = ui[2], i3 = ui[3];
    unsigned a, b, c, d;
    asm("v_fmac_f32 %0, %12, %15\n\tv_fmac_f32 %4, %12, %16\n\tv_fmac_f32 %0, %14, %16\n\tv_fmac_f32 %4, %13, %15\n\t"
        "v_fmac_f32 %1, %12, %0\n\tv_fmac_f32 %5, %12, %4\n\tv_fmac_f32 %1, %14, %4\n\tv_fmac_f32 %5, %13, %0\n\t"
        "v_fmac_f32 %2, %12, %1\n\tv_fmac_f32 %6, %12, %5\n\tv_fmac_f32 %2, %14, %5\n\tv_fmac_f32 %6, %13, %1\n\t"
        "v_fmac_f32 %3, %12, %2\n\tv_fmac_f32 %7, %12, %6\n\tv_fmac_f32 %3, %14, %6\n\tv_fmac_f32 %7, %13, %2\n\t"
        "v_cvt_pk_bf16_f32 %8, %0, %1\n\tv_cvt_pk_bf16_f32 %9, %2, %3\n\tv_cvt_pk_bf16_f32 %10, -%4, -%5\n\tv_cvt_pk_bf16_f32 %11, -%6, -%7"
        : "+v"(r0), "+v"(r1), "+v"(r2), "+v"(r3), "+v"(i0), "+v"(i1), "+v"(i2), "+v"(i3), "=&v"(a), "=&v"(b), "=&v"(c), "=&v"(d)
        : "v"(lr), "v"(li), "v"(nli), "v"(hr), "v"(hi));
    hr = r3; hi = i3; ore = (u32x2){a, b}; oim = (u32x2){c, d};
}
DI unsigned gload_u16(unsigned voff, const void* sbase) { unsigned r; asm volatile("global_load_ushort %0, %1, %2" : "=v"(r) : "v"(voff), "s"(sbase) : "memory"); return r; }
DI void cmul(float ar, float ai, float br, float bi, float& cr, float& ci) { cr = ar * br - ai * bi; ci = ar * bi + ai * br; }

DI void ssm_params(unsigned raw_dt, unsigned raw_re, unsigned raw_im, float& lr, float& li, float& cr, float& ci) {
    const float dt = rbf(expf(bf2f(raw_dt)));
    const float ar = bf2f(raw_re), ai = bf2f(raw_im);
    const float e = expf(dt * ar);
    float sn, cs;
    sincosf(dt * ai, &sn, &cs);
    lr = e * cs; li = e * sn;
    const float nr = lr - 1.0f, ni = li, den = ar * ar + ai * ai;
    cr = 2.0f * ((nr * ar + ni * ai) / den);
    ci = 2.0f * ((ni * ar - nr * ai) / den);
}
DI void pow20(float lr, float li, float& r, float& i) {
    float a2r, a2i, a4r, a4i, a5r, a5i, a10r, a10i;
    cmul(lr, li, lr, li, a2r, a2i); cmul(a2r, a2i, a2r, a2i, a4r, a4i); cmul(a4r, a4i, lr, li, a5r, a5i);
    cmul(a5r, a5i, a5r, a5i, a10r, a10i); cmul(a10r, a10i, a10r, a10i, r, i);
}

#define CSARG
#define CSPASS
DI void mfma_acc(f32x4& c, const bf16x8& a, const bf16x8& b) { asm volatile("v_mfma_f32_16x16x32_bf16 %0, %1, %2, %0" : "+v"(c) : "v"(a), "v"(b)); }
DI void set_m0(unsigned a, unsigned b) { asm volatile("s_add_i32 m0, %0, %1" ::"s"(a), "s"(b) : "memory", "scc"); }
template <int N> DI void set_m0i(unsigned a) { asm volatile("s_add_i32 m0, %0, %1" ::"s"(a), "n"(N) : "memory", "scc"); }
DI void glds_m0(unsigned voff, const void* sbase) {
    asm volatile("global_load_lds_dwordx4 %0, %1" ::"v"(voff), "s"(sbase) : "memory"); }
template <int OFF> DI void glds_m0_sc1(unsigned voff, const void* sbase) {
    asm volatile("global_load_lds_dwordx4 %0, %1 offset:%2 sc1" ::"v"(voff), "s"(sbase), "n"(OFF) : "memory");
}
template <int OFF> DI void glds_m0_half_sc1(unsigned voff, const void* sbase) {
    asm volatile("s_mov_b32 exec_hi, 0\n\tglobal_load_lds_dwordx4 %0, %1 offset:%2 sc1\n\ts_mov_b32 exec_hi, -1" ::"v"(voff), "s"(sbase), "n"(OFF) : "memory");
}
DI void glds_m0_half(unsigned voff, const void* sbase) {
    asm volatile("s_mov_b32 exec_hi, 0\n\tglobal_load_lds_dwordx4 %0, %1\n\ts_mov_b32 exec_hi, -1" ::"v"(voff), "s"(sbase) : "memory");
}
template <int N> DI void waitv0() { asm volatile("s_waitcnt vmcnt(%0)" ::"n"(N) : "memory"); }
template <int N> DI unsigned smul(unsigned a) { unsigned r; asm("s_mul_i32 %0, %1, %2" : "=s"(r) : "s"(a), "n"(N)); return r; }
#define LGKM0() __builtin_amdgcn_s_waitcnt(0xC07F)
DI unsigned opaque(unsigned v) { asm volatile("" : "+v"(v)); return v; }

DI void wt_request(const Params& p, int u, int w, int lane, unsigned buf) {
    const int s = u / 5, cc = u - 5 * s, row = 4 * w + (lane >> 4);
    glds16((unsigned)(32 * s + row) * 1280u + (unsigned)(256 * cc) + (unsigned)(((lane & 15) ^ ((row & 7) << 1)) * 16), p.wd, buf + w * 1024);
}
DI bf16x8 wt_read(int w, int lane, unsigned buf) {
    const int row = lane >> 2, pp = lane & 3;
    const unsigned a = buf + row * 256 + (((2 * w + (pp >> 1)) ^ ((row & 7) << 1)) * 16) + 8 * (pp & 1);
    return ldstr(a, a + 4096);
}
DI void wt_store(const Params& p, int u, int w, int lane, const bf16x8& v) {
    const int s = u / 5, cc = u - 5 * s, d16 = 8 * cc + w, tj = d16 / 5, n = d16 - 5 * tj;
    const __amdgpu_buffer_rsrc_t r = __builtin_amdgcn_make_buffer_rsrc(p.ws + WS_WT, 0, WT_BYTES, 0x00020000);
    __builtin_amdgcn_raw_buffer_store_b128(__builtin_bit_cast(u32x4, v), r, (unsigned)((tj * 14 + (s >> 2)) * 20 + (s & 3) * 5 + n) * 1024u + lane * 16, 0, 16);
}
DI unsigned wt_flag(int u) { return WS_TFLAG + ((u % 5) * 64 + u / 5) * 8; }

template <bool SSM, int TH, class HF, class H0, class H, class H2, class H3, class H4>
DI void p1_main(const Params& p, f32x4 (&acc)[5][4], int pan, int ct, int w, int lane, unsigned lds0, int unit, HF first, H0 gap, H pre, H2 warm, H3 late, H4 fin, unsigned long long& tag CSARG) {
    first();
    constexpr int th = TH;
    const int c = w & 3, t0 = pan * PM;
    constexpr int NX = TH ? 2 : 3, NP = NX + 4;
    unsigned xoff[3], woff[4];
#pragma unroll
    for (int i = 0; i < NX; ++i) {
        const int row = 8 * (w + 8 * i) + (lane >> 3);
        int tr = row;
        if (SSM) { const int hh = row >= 80 ? 1 : 0, rr = row - 80 * hh, b = rr >> 4, rho = rr & 15; tr = 80 * hh + 20 * (rho >> 2) + 4 * b + (rho & 3); }
        const int g = min(t0 + tr, L - 1);
        xoff[i] = (unsigned)g * 1280u + (unsigned)(((lane & 7) ^ ((row >> 1) & 7)) * 16);
    }
    const char* wb0; const char* wb1; unsigned wstep;
    if (!SSM) {
        const int kq = lane >> 4, swz = kq | (((w >> 2) & 1) << 2);
        int j = (lane & 15) ^ (swz << 1);
        if (ct == 13) j &= 7;
#pragma unroll
        for (int i = 0; i < 4; ++i) woff[i] = (unsigned)(4 * (w >> 1) + 16 * i + kq) * 3456u + (unsigned)(128 * ct + 8 * j) * 2u;
        wb0 = wb1 = (const char*)((w & 1) ? p.wu : p.wg);
        wstep = 64u * 3456u;
    } else {
        const int r = 8 * w + (lane >> 3);
#pragma unroll
        for (int i = 0; i < 4; ++i) woff[i] = (unsigned)(128 * ct + r + 64 * (i & 1)) * 1280u + (unsigned)(((lane & 7) ^ ((r >> 1) & 7)) * 16);
        wb0 = (const char*)p.bre; wb1 = (const char*)p.bim;
        wstep = 128u;
    }
    const char* xb = (const char*)p.x;
    const unsigned mX = lds0 + w * 1024, mW = lds0 + XS + w * 1024;
    auto stW = [&](unsigned slot, int i) { set_m0(slot, mW + i * 8192); asm volatile("s_nop 0"); glds_m0(woff[i], (SSM && i >= 2) ? wb1 : wb0); };
    auto stX = [&](unsigned slot, int i) { set_m0(slot, mX + i * 8192); asm volatile("s_nop 0"); glds_m0(xoff[i], xb); };
    const int rho = lane & 15, q = lane >> 4;
    const unsigned xa0 = lds0 + (unsigned)(80 * th + rho) * 128u + (unsigned)((q ^ ((rho >> 1) & 7)) * 16), xa1 = xa0 ^ 64u;
    unsigned wa0, wa1;
    if (!SSM) {
        const int qq = (lane & 15) >> 2, pp = lane & 3, x = (qq << 1) | ((q & 1) << 3);
        wa0 = lds0 + XS + q * 4096 + qq * 256 + (((4 * c + (pp >> 1)) ^ x) * 16) + 8 * (pp & 1);
        wa1 = wa0 ^ 32u;
    } else {
        wa0 = lds0 + XS + (unsigned)(32 * c + rho) * 128u + (unsigned)((q ^ ((rho >> 1) & 7)) * 16);
        wa1 = wa0 ^ 64u;
    }
    bf16x8 xf[2][5], wf[2][4];
    unsigned bx, bw0, bw1;
    auto bases = [&](unsigned slot, int s) {
        bx = opaque(slot + (s ? xa1 : xa0));
        if (!SSM) { bw0 = opaque(slot + wa0); bw1 = opaque(slot + wa1); } else bw0 = opaque(slot + (s ? wa1 : wa0));
    };
    auto rdW = [&](int set, int s, int i) {
        if (!SSM) { const unsigned a = ((i >> 1) ? bw1 : bw0) + s * 16384 + (i & 1) * 1024; wf[set][i] = ldstr(a, a + 2048); }
        else wf[set][i] = ldsr128(bw0 + (i >> 1) * 16384 + (i & 1) * 2048);
    };
    auto rdX = [&](int set, int b) {
        xf[set][b] = ldsr128(bx + b * 2048); };
    auto mm = [&](int set, int i) {
        if (SSM) mfma_acc(acc[i >> 2][i & 3], xf[set][i >> 2], wf[set][i & 3]); else mfma_acc(acc[i >> 2][i & 3], wf[set][i & 3], xf[set][i >> 2]);
    };
#define KST(i) do {} while (0)
    KST(22); CS(60);
    auto stage = [&](unsigned slot) {
#pragma unroll
        for (int i = 0; i < 4; ++i) stW(slot, i);
#pragma unroll
        for (int i = 0; i < NX; ++i) stX(slot, i);
        wb0 += wstep; wb1 += wstep; xb += 128;
    };
    wt_request(p, unit, w, lane, lds0 + 2 * STG);
    stage(0);
    SCHED();
    gap();
#pragma unroll
    for (int b = 0; b < 5; ++b)
#pragma unroll
        for (int blk = 0; blk < 4; ++blk) acc[b][blk] = (f32x4){0.f, 0.f, 0.f, 0.f};
    asm volatile("" : "+v"(acc[0][0]), "+v"(acc[0][1]), "+v"(acc[0][2]), "+v"(acc[0][3]), "+v"(acc[1][0]), "+v"(acc[1][1]), "+v"(acc[1][2]), "+v"(acc[1][3]),
                      "+v"(acc[2][0]), "+v"(acc[2][1]), "+v"(acc[2][2]), "+v"(acc[2][3]), "+v"(acc[3][0]), "+v"(acc[3][1]), "+v"(acc[3][2]), "+v"(acc[3][3]),
                      "+v"(acc[4][0]), "+v"(acc[4][1]), "+v"(acc[4][2]), "+v"(acc[4][3]));
    SCHED();
    stage(STG);
    CS(61);
    waitv0<2 * NP>();
    BAR();
    const bf16x8 wt = wt_read(w, lane, lds0 + 2 * STG);
    pre();
    waitv0<NP>(); CS(62);
    LGKM0(); BAR();
    if (TH == 1) __builtin_amdgcn_s_setprio(1);
    bases(0, 0);
#pragma unroll
    for (int i = 0; i < 4; ++i) rdW(0, 0, i);
#pragma unroll
    for (int i = 0; i < 5; ++i) rdX(0, i);
    bases(0, 1);
    if (TH == 1) stage(2 * STG);
    wt_store(p, unit, w, lane, wt);
    KST(0); CS(63);
    unsigned cur = 0, nx1 = STG, nx2 = 2 * STG;
    auto body = [&](auto kind, int t) {
        constexpr int K = decltype(kind)::value;
        if (K == 3) late();
        LGKM0(); SCHED();
#pragma unroll
        for (int i = 0; i < 20; ++i) {
            mm(0, i);
            if (i < 4) rdW(1, 1, i);
            else if (i < 9) rdX(1, i - 4);
            if (TH == 0 && K < 2 && i >= 5 && i < 19) {
                const int k = (i - 5) >> 1;
                if (((i - 5) & 1) == 0) set_m0(nx2, k < 4 ? mW + k * 8192 : mX + (k - 4) * 8192);
                else if (k < 4) glds_m0(woff[k], (SSM && k >= 2) ? wb1 : wb0);
                else glds_m0(xoff[k - 4], xb);
            }
            if (K < 3 && i == 19) bases(nx1, 0);
            SCHED();
        }
        if (TH == 0 && K < 2) { wb0 += wstep; wb1 += wstep; xb += 128; }
        if (K < 2) waitv0<NP>(); else if (K == 2) waitv0<TH>();
        KST(2 * t + 1);
        LGKM0(); BAR(); SCHED();
        KST(2 * t + 2);
        if (TH == 1 && K == 0) { stage(cur); SCHED(); }
        if (TH == 1 && K == 1) { warm(); SCHED(); }
#pragma unroll
        for (int i = 0; i < 20; ++i) {
            mm(1, i);
            if (K < 3) {
                if (i < 4) rdW(0, 0, i);
                else if (i < 9) rdX(0, i - 4);
                else if (i == 18) bases(nx1, 1);
            }
            SCHED();
        }
        const unsigned o = cur; cur = nx1; nx1 = nx2; nx2 = o;
    };
#pragma unroll 1
    for (int t = 0; t < 7; ++t) body(IC<0>{}, t);
    if (w == 0) __hip_atomic_store((GAS unsigned long long*)(p.ws + wt_flag(unit)), tag, RLX);
    body(IC<1>{}, 7); body(IC<2>{}, 8); body(IC<3>{}, 9);
    if (TH == 1) __builtin_amdgcn_s_setprio(0);
    asm volatile("s_nop 7\n\ts_nop 7"
                 : "+v"(acc[0][0]), "+v"(acc[0][1]), "+v"(acc[0][2]), "+v"(acc[0][3]), "+v"(acc[1][0]), "+v"(acc[1][1]), "+v"(acc[1][2]), "+v"(acc[1][3]),
                   "+v"(acc[2][0]), "+v"(acc[2][1]), "+v"(acc[2][2]), "+v"(acc[2][3]), "+v"(acc[3][0]), "+v"(acc[3][1]), "+v"(acc[3][2]), "+v"(acc[3][3]),
                   "+v"(acc[4][0]), "+v"(acc[4][1]), "+v"(acc[4][2]), "+v"(acc[4][3])::"memory");
    fin();
    KST(21);
}

template <int N> DI void waitv() { asm volatile("s_waitcnt vmcnt(%0)" ::"n"(N) : "memory"); }
constexpr int p2_pos(int k, int e) { return k == 1 ? 0 : k == 2 ? 1 : k == 3 ? 2 : k == 4 ? 3 : 2 * (k - 3) + e; }
constexpr int p2_nw(int NT, bool down, int T) {
    return (T + (int)NSLOT < NT || (!down && T == 3)) ? 3 : 0;
}
constexpr int p2_younger(int NT, bool down, int NX, int T, int e) {
    int n = 0; bool seen = false;
    if (T == 0 && e == 0) { seen = true; n = 2; } else if (T == 0) seen = true;
    if (seen) n += 3 * ((int)NSLOT - NEARLY);
    else {   }
    for (int t = 0; t < NT; ++t) {
        if (t == NT - 2 && seen) n += NX;
        if (t == T && e == 0) return n;
        if (down && t == 1 && seen) n += 1;
        for (int k = 1; k < NT; ++k) for (int h = 0; h < 2; ++h)
            if (p2_pos(k, h) == 2 * t) { if (seen) n += 2; if (k == T && h == e) seen = true; }
        if (t == T && e == 1) return n;
        if (seen) n += p2_nw(NT, down, t);
        for (int k = 1; k < NT; ++k) for (int h = 0; h < 2; ++h)
            if (p2_pos(k, h) == 2 * t + 1) { if (seen) n += 2; if (k == T && h == e) seen = true; }
    }
    return n;
}
struct W2Src { unsigned vo[3]; };
template <bool DOWN> DI W2Src w2_src(int tj, int w, int lane) {
    W2Src r;
#pragma unroll
    for (int i = 0; i < 3; ++i) {
        if (DOWN) r.vo[i] = (unsigned)(2560 * w + lane * 16);
        else {
            const int row = min(20 * w + 8 * i + (lane >> 3), 159), hi = row >= 80 ? 1 : 0;
            r.vo[i] = (unsigned)(80 * tj + row - 80 * hi) * 1280u + (unsigned)(128 * hi) + (unsigned)(((lane & 7) ^ ((row >> 1) & 7)) * 16);
        }
    }
    return r;
}
template <bool DOWN> DI void w2_piece(const Params& p, const W2Src& s, int T, int tj, int w, int i) {
    if (DOWN) {
        const char* base = p.ws + WS_WT + (unsigned)(tj * 14 + T) * 20480u;
        set_m0(smul<2560>(w), (unsigned)((T % NSLOT) * S2));
        asm volatile("s_nop 0");
        if (i == 0) glds_m0_sc1<0>(s.vo[0], base); else if (i == 1) glds_m0_sc1<1024>(s.vo[0], base); else glds_m0_half_sc1<2048>(s.vo[0], base);
    } else {
        const char* base = (const char*)(T >= 5 ? p.cim : p.cre) + (unsigned)(T >= 5 ? T - 5 : T) * 256u;
        set_m0(smul<2560>(w), (unsigned)((T % NSLOT) * S2 + i * 1024));
        asm volatile("s_nop 0");
        if (i < 2) glds_m0(s.vo[i], base); else glds_m0_half(s.vo[i], base);
    }
}
DI const char* c_warm_addr(const Params& p, int b, int w, int lane) {
    const char *cre = (const char*)p.cre, *cim = (const char*)p.cim;
    asm volatile("" : "+v"(lane), "+s"(cre), "+s"(cim));
    const int m = min(((b >> 3) * 4 + (w & 3)) * 50 + min(lane, 49), 6399), im = m >= 3200 ? 1 : 0, r = m - 3200 * im, row = r / 10;
    return (im ? cim : cre) + (320 * (b & 1) + row) * 1280 + 128 * (r - 10 * row);
}
DI void l2_warm(const char* a) { asm volatile("s_mov_b32 m0, %1\n\ts_nop 0\n\tglobal_load_lds_dword %0, off" ::"v"(a), "s"(LDS_JUNK) : "memory"); }
DI void x_piece(const Params& p, int ti, int tj, int w, int lane, int i) {
    asm volatile("" : "+v"(lane));
    const int idx = min(160 * w + 64 * i + lane, 1279), row = idx / 10, gr = idx - 10 * row;
    set_m0(smul<2560>(w), (unsigned)(3 * S2 + i * 1024));
    asm volatile("s_nop 0");
    const unsigned vo = (unsigned)(128 * ti + row) * 1280u + (unsigned)(80 * tj + 8 * gr) * 2u;
    if (i < 2) glds_m0(vo, p.x); else glds_m0_half(vo, p.x);
}
template <int N> DI void check_flags(u32x2& f, unsigned long long tag, unsigned voff, const char* base) {
    unsigned cnt;
    asm volatile("s_waitcnt vmcnt(%5)\n\t"
                 "v_cmp_eq_u64 vcc, %0, %2\n\t"
                 "s_cmp_eq_u64 vcc, exec\n\t"
                 "s_cbranch_scc1 2f\n\t"
                 "s_mov_b32 %1, 0\n"
                 "1:\n\t"
                 "global_load_dwordx2 %0, %3, %4 sc1\n\t"
                 "s_waitcnt vmcnt(0)\n\t"
                 "v_cmp_eq_u64 vcc, %0, %2\n\t"
                 "s_cmp_eq_u64 vcc, exec\n\t"
                 "s_cbranch_scc1 2f\n\t"
                 "s_add_u32 %1, %1, 1\n\t"
                 "s_cmp_gt_u32 %1, 0x10000\n\t"
                 "s_cbranch_scc1 2f\n\t"
                 "s_sleep 1\n\t"
                 "s_branch 1b\n"
                 "2:"
                 : "+v"(f), "=&s"(cnt)
                 : "v"(tag), "v"(voff), "s"(base), "n"(N)
                 : "vcc", "scc", "memory");
}
DI void tie2(u32x4& a, u32x4& b) { asm volatile("; TIE %0 %1" : "+v"(a), "+v"(b)); }

template <bool DOWN, int NEXTRA, class H> DI void p2_loop(const Params& p, f32x4 (&acc)[2][5], const W2Src& src, int ti, int tj, int w, int lane, unsigned lds0, H extra, unsigned long long tag CSARG) {
    constexpr int NT = DOWN ? 14 : 10, TX = NT - 2;
    const int tg = w & 3, kh = w >> 2;
    const char* a2 = p.ws + WS_A2 + (unsigned)((DOWN ? 0 : KSF) + 2 * kh) * 1024u;
    unsigned va[2];
#pragma unroll
    for (int m = 0; m < 2; ++m) va[m] = (unsigned)((8 * ti + 2 * tg + (m ^ kh)) * KS2) * 1024u + lane * 16;
    u32x4 A[3][2][2];
    auto ldA = [&](int T, int e) {
#pragma unroll
        for (int m = 0; m < 2; ++m) A[T % 3][e][m] = gload_sc1(va[m], a2 + (4 * T + e) * 1024);
    };
    ldA(0, 0); ldA(0, 1);
    unsigned lb0, lb1;
    if (DOWN) { lb0 = lds0 + (unsigned)(10240 * kh + lane * 16); lb1 = lb0 + 5120; }
    else { const int rho = lane & 15, q = lane >> 4; lb0 = lds0 + (unsigned)(80 * kh + rho) * 128u + (unsigned)((q ^ ((rho >> 1) & 7)) * 16); lb1 = lb0 ^ 64u; }
    bf16x8 wf[2][5];
    unsigned rb;
    auto rdW = [&](int set, int n) { wf[set][n] = ldsr128(rb + n * (DOWN ? 1024 : 2048)); };
#pragma unroll
    for (int m = 0; m < 2; ++m)
#pragma unroll
        for (int n = 0; n < 5; ++n) acc[m][n] = (f32x4){0.f, 0.f, 0.f, 0.f};
    asm volatile("" : "+v"(acc[0][0]), "+v"(acc[0][1]), "+v"(acc[0][2]), "+v"(acc[0][3]), "+v"(acc[0][4]), "+v"(acc[1][0]), "+v"(acc[1][1]), "+v"(acc[1][2]),
                      "+v"(acc[1][3]), "+v"(acc[1][4]));
    rb = opaque(lb0);
#pragma unroll
    for (int n = 0; n < 5; ++n) rdW(0, n);
    SCHED();
#pragma unroll
    for (int k = 3 * NEARLY; k < 3 * (int)NSLOT; ++k) w2_piece<DOWN>(p, src, k / 3, tj, w, k % 3);
    u32x2 lf;
    const int lk = lane & 15;
    const unsigned lfo = WS_FLAG + ((((lane >> 4) & 1) ? (128 * ti + 127) / PM : (128 * ti) / PM) * 32 + 5 + (lk < 9 ? lk : 0)) * 8;
    auto issue = [&](int pos) {
        if (DOWN && pos == 2) asm volatile("global_load_dwordx2 %0, %1, %2 sc1" : "=v"(lf) : "v"(lfo), "s"(p.ws) : "memory");
        if (DOWN && pos == 4) check_flags<11>(lf, tag, lfo, p.ws);
#pragma unroll
        for (int k = 1; k < NT; ++k)
#pragma unroll
            for (int h = 0; h < 2; ++h) if (p2_pos(k, h) == pos) ldA(k, h);
    };
    auto step = [&](auto tc) {
        constexpr int T = decltype(tc)::value;
        const bool live = !(DOWN && T == 13) || kh == 0;
        if (T == TX) extra();
        if (DOWN && T == 13) { waitv<NEXTRA>(); tie2(A[T % 3][1][0], A[T % 3][1][1]); }
        waitv<p2_younger(NT, DOWN, NEXTRA, T, 0)>(); tie2(A[T % 3][0][0], A[T % 3][0][1]);
        rb = opaque(lb1 + (T % NSLOT) * S2);
        LGKM0(); SCHED();
        if (live) {
#pragma unroll
            for (int i = 0; i < 10; ++i) {
                mfma_acc(acc[i / 5][i % 5], wf[0][i % 5], __builtin_bit_cast(bf16x8, A[T % 3][0][i / 5]));
                if (i < 5) rdW(1, i);
                SCHED();
            }
        }
        issue(2 * T);
        waitv<p2_younger(NT, DOWN, NEXTRA, T, 1)>(); tie2(A[T % 3][1][0], A[T % 3][1][1]);
        LGKM0(); BAR(); SCHED();
        if (T + 1 < NT) rb = opaque(lb0 + ((T + 1) % NSLOT) * S2);
        if (live) {
#pragma unroll
            for (int i = 0; i < 10; ++i) {
                mfma_acc(acc[i / 5][i % 5], wf[1][i % 5], __builtin_bit_cast(bf16x8, A[T % 3][1][i / 5]));
                if (T + 1 < NT && i < 5) rdW(0, i);
                if (T + (int)NSLOT < NT && i >= 5 && i < 8) w2_piece<DOWN>(p, src, T + NSLOT, tj, w, i - 5);
                if (!DOWN && T == 3 && i >= 5 && i < 8) x_piece(p, ti, tj, w, lane, i - 5);
                SCHED();
            }
        }
        issue(2 * T + 1);
        SCHED();
    };
    step(IC<0>{}); step(IC<1>{}); step(IC<2>{}); step(IC<3>{}); step(IC<4>{}); step(IC<5>{}); step(IC<6>{}); step(IC<7>{}); step(IC<8>{}); step(IC<9>{});
    if (DOWN) { step(IC<10>{}); step(IC<11>{}); step(IC<12>{}); step(IC<13>{}); }
    asm volatile("s_nop 7\n\ts_nop 7"
                 : "+v"(acc[0][0]), "+v"(acc[0][1]), "+v"(acc[0][2]), "+v"(acc[0][3]), "+v"(acc[0][4]), "+v"(acc[1][0]), "+v"(acc[1][1]), "+v"(acc[1][2]),
                   "+v"(acc[1][3]), "+v"(acc[1][4])::"memory");
}
DI void p2_join(f32x4 (&acc)[2][5], int w, int lane, unsigned lds0) {
    const unsigned xw = lds0 + w * 5120 + lane * 16, xr = lds0 + (w ^ 4) * 5120 + lane * 16;
#pragma unroll
    for (int n = 0; n < 5; ++n) *(LAS f32x4*)(size_t)(xw + n * 1024) = acc[1][n];
    WAITL0(); BAR();
#pragma unroll
    for (int n = 0; n < 5; ++n) acc[0][n] += *(const LAS f32x4*)(size_t)(xr + n * 1024);
}
static_assert(p2_younger(14, true, 0, 0, 0) == 2 + 3 * (NSLOT - NEARLY) && p2_younger(14, true, 0, 2, 1) == 14 && p2_younger(14, true, 0, 6, 0) == 19 && p2_younger(14, true, 0, 6, 1) == 16 && p2_younger(10, false, 0, 9, 1) == 0 && NEARLY >= 4, "vmcnt schedule");

DI unsigned long long ld_flag(const char* ws, unsigned off) { return __hip_atomic_load((GAS unsigned long long*)(ws + off), RLX); }

DI void spin_flags(const char* ws, unsigned off, unsigned long long tag) {
    for (unsigned spins = 0;; ++spins) {
        if (__all(ld_flag(ws, off) == tag) || spins > SPIN_CAP) break;
        __builtin_amdgcn_s_sleep(1);
    }
    asm volatile("" ::: "memory");
}

__global__ __launch_bounds__(512, 2) void s5_swiglu_mix_kernel(Params p) {
    __shared__ __attribute__((aligned(1024))) char lds[LDS_BYTES];
    const int tid = threadIdx.x, lane = tid & 63;
    const int w = __builtin_amdgcn_readfirstlane(tid >> 6);
    const int b = blockIdx.x;
    const unsigned lds0 = (unsigned)(size_t)(LAS char*)lds;
    CS(0);
    STAMP(0);
    const unsigned job = p.job[b];
    u32x2 e_in;
    auto ask_tag = [&]() { asm volatile("global_load_dwordx2 %0, %1, %2 sc1" : "=v"(e_in) : "v"(0u), "s"(p.ws + WS_EPOCH) : "memory"); };
    unsigned tag0 = 0, tag1 = 0;
    unsigned long long tag = 0;
    auto take_tag = [&]() { asm volatile("; TIE %0" : "+v"(e_in)); tag0 = e_in[0] + 1u; tag1 = e_in[1] + 1u; tag = ((unsigned long long)tag1 << 32) | tag0; };
    const __amdgpu_buffer_rsrc_t a2r = __builtin_amdgcn_make_buffer_rsrc(p.ws + WS_A2, 0, A2_BYTES, 0x00020000);

    const int role = job & 3;
    const int pan = (job >> 2) & 15, ct = (job >> 6) & 15;
    const bool down = (job >> 10) & 1;
    const int pair = job >> 11, ti = pair >> 3, tj = pair & 7;
    const unsigned flag_off = WS_FLAG + (role == 0 ? pan * 32 + ct : role == 1 ? pan * 32 + 16 + ct : role == 2 ? 13 * 32 + pan : 13 * 32 + 8) * 8;
    W2Src src;
    auto make_src = [&]() { int l2 = lane; asm volatile("" : "+v"(l2)); src = down ? w2_src<true>(tj, w, l2) : w2_src<false>(tj, w, l2); };

    const char* warm_a;
    auto setup = [&]() { make_src(); warm_a = c_warm_addr(p, b, w, lane); };
    auto warm = [&]() { l2_warm(warm_a); };
    const unsigned tf0 = WS_TFLAG + (((5 * tj) >> 3) * 64 + min(lane, 53)) * 8, tf1 = WS_TFLAG + (((5 * tj + 4) >> 3) * 64 + min(lane, 53)) * 8;
    u32x2 tfa, tfb;
    auto tf_request = [&]() {
        asm volatile("global_load_dwordx2 %0, %2, %4 sc1\n\tglobal_load_dwordx2 %1, %3, %4 sc1" : "=&v"(tfa), "=&v"(tfb) : "v"(tf0), "v"(tf1), "s"(p.ws) : "memory");
    };
    bool tf_ok = false;
    auto tf_take = [&]() {
        asm volatile("; TIE %0 %1" : "+v"(tfa), "+v"(tfb));
        tf_ok = __all(tfa[0] == tag0 && tfa[1] == tag1 && tfb[0] == tag0 && tfb[1] == tag1);
    };

    if (role == 0) {
        f32x4 acc[5][4];
        auto fin = [&]() { WAITV(0); tf_take(); };
        if (w < 4) p1_main<false, 0>(p, acc, pan, ct, w, lane, lds0, b, ask_tag, setup, take_tag, warm, tf_request, fin, tag CSPASS);
        else p1_main<false, 1>(p, acc, pan, ct, w, lane, lds0, b, ask_tag, setup, take_tag, warm, tf_request, fin, tag CSPASS);
        if (down && !tf_ok) { spin_flags(p.ws, tf0, tag); spin_flags(p.ws, tf1, tag); }
        STAMP(1); CS(1);
        const int th = w >> 2, c = w & 3, s2 = 4 * ct + c;
        auto epi = [&](auto dn) {
            constexpr bool DN = decltype(dn)::value != 0;
            auto w2_pc = [&](int k) { w2_piece<DN>(p, src, k / 3, tj, w, k % 3); };
#pragma unroll
            for (int bb = 0; bb < 5; ++bb) {
                float a[8];
#pragma unroll
                for (int k = 0; k < 8; ++k) a[k] = __builtin_amdgcn_exp2f(acc[bb][2 * (k >> 2)][k & 3] * -1.44269504088896f);
                if (2 * bb < NE1) w2_pc(2 * bb);
                SCHED();
#pragma unroll
                for (int k = 0; k < 8; ++k) a[k] = __builtin_amdgcn_rcpf(1.0f + a[k]);
                if (2 * bb + 1 < NE1) w2_pc(2 * bb + 1);
                SCHED();
#pragma unroll
                for (int k = 0; k < 8; ++k) a[k] = acc[bb][2 * (k >> 2)][k & 3] * a[k] * acc[bb][2 * (k >> 2) + 1][k & 3];
                const u32x4 v = {pack2(a[0], a[1]), pack2(a[2], a[3]), pack2(a[4], a[5]), pack2(a[6], a[7])};
                const int tb = 10 * pan + 5 * th + bb;
                if (tb < 128 && s2 < KSF) __builtin_amdgcn_raw_buffer_store_b128(v, a2r, (unsigned)(tb * KS2 + s2) * 1024u + lane * 16, 0, 16);
                SCHED();
            }
            STAMP(2); CS(2);
#pragma unroll
            for (int k = NE1; k < NE1 + NE2; ++k) w2_pc(k);
            waitv<NE2>(); CS(3); BAR(); CS(4);
            if (w == 0) __hip_atomic_store((GAS unsigned long long*)(p.ws + flag_off), tag, RLX);
#pragma unroll
            for (int k = NE1 + NE2; k < 3 * NEARLY; ++k) w2_pc(k);
        };
        if (down) epi(IC<1>{}); else epi(IC<0>{});
        STAMP(3);
    } else if (role == 1) {
        const int th = w >> 2, c = w & 3, sg = lane & 15, q = lane >> 4;
        float t20r = 0.f, t20i = 0.f, t160r = 0.f, t160i = 0.f;
        unsigned raw[3];
        auto ask_params = [&]() {
            ask_tag();
            const unsigned st = (unsigned)(128 * ct + (tid & 127)) * 2u;
            raw[0] = gload_u16(st, p.logdt); raw[1] = gload_u16(st, p.lre); raw[2] = gload_u16(st, p.lim);
        };
        f32x4 acc[5][4];
        auto params = [&]() {
            take_tag();
            asm volatile("; TIE %0 %1 %2" : "+v"(raw[0]), "+v"(raw[1]), "+v"(raw[2]));
            if (w < 2) {
                float a, bq, cc, dd, xr, xi, yr, yi;
                ssm_params(raw[0], raw[1], raw[2], a, bq, cc, dd);
                *(LAS f32x4*)(size_t)(lds0 + LDS_TAB + tid * 16) = (f32x4){a, bq, cc, dd};
                pow20(a, bq, t20r, t20i);
                cmul(t20r, t20i, t20r, t20i, xr, xi); cmul(xr, xi, xr, xi, yr, yi); cmul(yr, yi, yr, yi, t160r, t160i);
                WAITL0();
            }
        };
        if (w < 4) p1_main<true, 0>(p, acc, pan, ct, w, lane, lds0, b, ask_params, setup, params, warm, []() {}, []() {}, tag CSPASS);
        else p1_main<true, 1>(p, acc, pan, ct, w, lane, lds0, b, ask_tag, setup, take_tag, warm, []() {}, []() {}, tag CSPASS);
        STAMP(1); CS(1);
        float lr[2], li[2], cr[2], ci[2];
#pragma unroll
        for (int n = 0; n < 2; ++n) {
            const f32x4 t = *(const LAS f32x4*)(size_t)(lds0 + LDS_TAB + (32 * c + 16 * n + sg) * 16);
            lr[n] = t[0]; li[n] = t[1]; cr[n] = t[2]; ci[n] = t[3];
        }
        const int seg = 4 * th + q;
        const unsigned ea = lds0 + LDS_E + (seg * 128 + 32 * c + sg) * 8;
        const float nli[2] = {-li[0], -li[1]}, nci[2] = {-ci[0], -ci[1]};
        {
            float er0 = 0.f, ei0 = 0.f, er1 = 0.f, ei1 = 0.f;
#pragma unroll
            for (int i = 0; i < 20; i += 2)
                horner2(er0, ei0, er1, ei1, lr[0], li[0], nli[0], lr[1], li[1], nli[1],
                        acc[i >> 2][0][i & 3], acc[i >> 2][2][i & 3], acc[i >> 2][1][i & 3], acc[i >> 2][3][i & 3],
                        acc[i >> 2][0][(i & 3) + 1], acc[i >> 2][2][(i & 3) + 1], acc[i >> 2][1][(i & 3) + 1], acc[i >> 2][3][(i & 3) + 1]);
            *(LAS f32x2*)(size_t)ea = (f32x2){cr[0] * er0 - ci[0] * ei0, cr[0] * ei0 + ci[0] * er0};
            *(LAS f32x2*)(size_t)(ea + 128) = (f32x2){cr[1] * er1 - ci[1] * ei1, cr[1] * ei1 + ci[1] * er1};
        }
        CS(5); WAITL0(); BAR(); CS(6);
        float pr[8], pi[8];
        if (w < 2) {
            float tr = 0.f, tii = 0.f;
#pragma unroll
            for (int j = 0; j < 8; ++j) {
                const f32x2 e = *(const LAS f32x2*)(size_t)(lds0 + LDS_E + (j * 128 + tid) * 8);
                pr[j] = tr; pi[j] = tii;
                const float t = t20r * tr - t20i * tii + e[0];
                tii = t20r * tii + t20i * tr + e[1]; tr = t;
            }
            if (pan < NPAN - 1) {
                const u32x4 gv = {__float_as_uint(tr), tag0, __float_as_uint(tii), tag1};
                const __amdgpu_buffer_rsrc_t gr = __builtin_amdgcn_make_buffer_rsrc(p.ws + WS_GRAN, 0, 12 * 640 * 16, 0x00020000);
                __builtin_amdgcn_raw_buffer_store_b128(gv, gr, (unsigned)(pan * 640 + 128 * ct + tid) * 16u, 0, 16);
            }
        }
        CS(7);
#pragma unroll
        for (int k = 0; k < 10; ++k) {
            const int n = k & 1, bb = k >> 1;
            cscale4(acc[bb][n], acc[bb][2 + n], cr[n], ci[n], nci[n]);
            if (k < 9 && w >= 2) w2_piece<false>(p, src, k / 3, tj, w, k % 3);
            SCHED();
        }
        STAMP(4); CS(8);
        if (w < 2) {
            float hr = 0.f, hi = 0.f;
            if (pan > 0) {
                const __amdgpu_buffer_rsrc_t gr = __builtin_amdgcn_make_buffer_rsrc(p.ws + WS_GRAN, 0, 12 * 640 * 16, 0x00020000);
                u32x4 gv[12];
                for (unsigned spins = 0;; ++spins) {
                    asm volatile("" ::: "memory");
                    bool ok = true;
#pragma unroll
                    for (int j = 0; j < 12; ++j) {
                        const int jj = j < pan ? j : pan - 1;
                        gv[j] = __builtin_amdgcn_raw_buffer_load_b128(gr, (unsigned)(jj * 640 + 128 * ct + tid) * 16u, 0, 16);
                    }
#pragma unroll
                    for (int j = 0; j < 12; ++j) ok &= (gv[j][1] == tag0) & (gv[j][3] == tag1);
                    if (__all(ok) || spins > SPIN_CAP) break;
                    __builtin_amdgcn_s_sleep(1);
                }
#pragma unroll
                for (int j = 0; j < 12; ++j) {
                    if (j < pan) {
                        const float t = t160r * hr - t160i * hi + __uint_as_float(gv[j][0]);
                        hi = t160r * hi + t160i * hr + __uint_as_float(gv[j][2]); hr = t;
                    }
                }
            }
#pragma unroll
            for (int j = 0; j < 8; ++j) {
                *(LAS f32x2*)(size_t)(lds0 + LDS_E + (j * 128 + tid) * 8) = (f32x2){pr[j] + hr, pi[j] + hi};
                const float t = t20r * hr - t20i * hi;
                hi = t20r * hi + t20i * hr; hr = t;
            }
#pragma unroll
            for (int k = 0; k < 9; ++k) w2_piece<false>(p, src, k / 3, tj, w, k % 3);
        }
        CS(9); WAITL0(); BAR();
        STAMP(5); CS(10);
        const unsigned trb = lds0 + LDS_TR + w * TR_WAVE;
        {
            f32x2 cw[2];
#pragma unroll
            for (int n = 0; n < 2; ++n) cw[n] = *(const LAS f32x2*)(size_t)(ea + n * 128);
            float hr[2] = {cw[0][0], cw[1][0]}, hi[2] = {cw[0][1], cw[1][1]};
            const unsigned ta = trb + sg * TR_STRIDE + 40 * q;
#pragma unroll
            for (int bb = 0; bb < 5; ++bb) {
#pragma unroll
                for (int n = 0; n < 2; ++n) {
                    u32x2 ore, oim;
                    scan4(hr[n], hi[n], acc[bb][n], acc[bb][2 + n], lr[n], li[n], nli[n], ore, oim);
                    const unsigned a = ta + 16 * n * TR_STRIDE + 8 * bb;
                    *(LAS u32x2*)(size_t)a = ore;
                    *(LAS u32x2*)(size_t)(a + 32 * TR_STRIDE) = oim;
                }
            }
        }
        WAITL0(); CS(11);
        {
            const int qq = (lane & 15) >> 2, pp = lane & 3;
            const unsigned rb = trb + (8 * q + qq) * TR_STRIDE + 8 * pp;
#pragma unroll
            for (int comp = 0; comp < 2; ++comp)
#pragma unroll
                for (int m = 0; m < 5; ++m) {
                    const unsigned a = rb + comp * 32 * TR_STRIDE + m * 32;
                    const bf16x8 v = ldstr(a, a + 4 * TR_STRIDE);
                    const int tb = 10 * pan + 5 * th + m, s2 = KSF + comp * 20 + 4 * ct + c;
                    if (tb < 128) __builtin_amdgcn_raw_buffer_store_b128(__builtin_bit_cast(u32x4, v), a2r, (unsigned)(tb * KS2 + s2) * 1024u + lane * 16, 0, 16);
                }
        }
        STAMP(2); CS(2);
        WAITV(0); CS(3); WAITL0(); BAR(); CS(4);
        if (w == 0) __hip_atomic_store((GAS unsigned long long*)(p.ws + flag_off), tag, RLX);
#pragma unroll
        for (int k = 9; k < 3 * NEARLY; ++k) w2_piece<false>(p, src, k / 3, tj, w, k % 3);
        STAMP(3);
    } else {
        setup();
        if (w >= 4) warm();
        ask_tag();
        WAITV(0); take_tag();
        const int nu = (role == 2 && pan < 7) ? 3 : 1;
        for (int k = 0; k < nu; ++k) wt_request(p, k ? 255 + 2 * pan + k : b, w, lane, lds0 + k * 8192);
        WAITV(0);
        BAR();
        for (int k = 0; k < nu; ++k) { const bf16x8 v = wt_read(w, lane, lds0 + k * 8192); wt_store(p, k ? 255 + 2 * pan + k : b, w, lane, v); }
        WAITV(0); WAITL0(); BAR();
        if (w == 0) for (int k = 0; k < nu; ++k) __hip_atomic_store((GAS unsigned long long*)(p.ws + wt_flag(k ? 255 + 2 * pan + k : b)), tag, RLX);
        if (role == 2) {
            const int i16 = lane & 15, rr = lane >> 4;
            u32x4 gw[5];
#pragma unroll
            for (int k = 0; k < 5; ++k) gw[k] = *(const u32x4*)(p.gw + 8 * (i16 + 16 * k));
            float mine = 0.f;
#pragma unroll
            for (int hf = 0; hf < 2; ++hf) {
                u32x4 xv[4][5];
#pragma unroll
                for (int ps = 0; ps < 4; ++ps)
#pragma unroll
                    for (int k = 0; k < 5; ++k) xv[ps][k] = *(const u32x4*)(p.x + (256 * pan + 32 * w + 16 * hf + 4 * ps + rr) * D + 8 * (i16 + 16 * k));
#pragma unroll
                for (int ps = 0; ps < 4; ++ps) {
                    float s = 0.f;
#pragma unroll
                    for (int k = 0; k < 5; ++k)
#pragma unroll
                        for (int e = 0; e < 4; ++e) {
                            const unsigned pk = pack2(bflo(xv[ps][k][e]) * bflo(gw[k][e]), bfhi(xv[ps][k][e]) * bfhi(gw[k][e]));
                            s += bflo(pk); s += bfhi(pk);
                        }
                    s += __shfl_xor(s, 8); s += __shfl_xor(s, 4); s += __shfl_xor(s, 2); s += __shfl_xor(s, 1);
                    const float z = rbf(s), g = rbf(1.0f / (1.0f + expf(-z)));
                    const float gsel = __shfl(g, (lane & 3) * 16);
                    if ((lane >> 2) == 4 * hf + ps) mine = gsel;
                }
            }
            if (lane < 32) {
                const __amdgpu_buffer_rsrc_t gr = __builtin_amdgcn_make_buffer_rsrc(p.ws + WS_GATE, 0, 2048 * 16, 0x00020000);
                __builtin_amdgcn_raw_buffer_store_b128((u32x4){__float_as_uint(mine), tag0, 0u, tag1}, gr, (unsigned)(256 * pan + 32 * w + lane) * 16u, 0, 16);
            }
        }
        WAITV(0); WAITL0(); BAR();
        if (w == 0) __hip_atomic_store((GAS unsigned long long*)(p.ws + flag_off), tag, RLX);
        if (down) {
            spin_flags(p.ws, tf0, tag); spin_flags(p.ws, tf1, tag);
#pragma unroll
            for (int k = 0; k < 3 * NEARLY; ++k) w2_piece<true>(p, src, k / 3, tj, w, k % 3);
        } else {
#pragma unroll
            for (int k = 0; k < 3 * NEARLY; ++k) w2_piece<false>(p, src, k / 3, tj, w, k % 3);
        }
        STAMP(3);
    }
    CS(12);
    if (w == 0) {
        const int p0 = (128 * ti) / PM, p1 = (128 * ti + 127) / PM, k = lane & 15, n = 5;
        spin_flags(p.ws, WS_FLAG + (((lane >> 4) & 1 ? p1 : p0) * 32 + (down ? 0 : 16) + (k < n ? k : 0)) * 8, tag);
    }
    WAITV(0);
    BAR();
    STAMP(6); CS(13);
    const int tb = 8 * ti + 2 * (w & 3) + (w >> 2), t = 16 * tb + (lane & 15), q = lane >> 4;
    u32x4 gq;
    f32x4 acc2[2][5];
    const unsigned vbase = (unsigned)(pair * 8 + (tb & 7)) * 5120u, voff = vbase + lane * 16;
    const __amdgpu_buffer_rsrc_t vr = __builtin_amdgcn_make_buffer_rsrc(p.ws + WS_V, 0, V_BYTES, 0x00020000);
    const int d0 = 80 * tj + 4 * q;
    auto gate = [&]() {
        if (!__all(gq[1] == tag0 && gq[3] == tag1)) {
            const __amdgpu_buffer_rsrc_t gr = __builtin_amdgcn_make_buffer_rsrc(p.ws + WS_GATE, 0, 2048 * 16, 0x00020000);
            for (unsigned spins = 0;; ++spins) {
                asm volatile("" ::: "memory");
                gq = __builtin_amdgcn_raw_buffer_load_b128(gr, (unsigned)t * 16u, 0, 16);
                if (__all(gq[1] == tag0 && gq[3] == tag1) || spins > SPIN_CAP) break;
                __builtin_amdgcn_s_sleep(1);
            }
        }
        return __uint_as_float(gq[0]);
    };
    if (down) {
        p2_loop<true, 1>(p, acc2, src, ti, tj, w, lane, lds0, [&]() { gq = gload_sc1((unsigned)t * 16u, p.ws + WS_GATE); }, tag CSPASS);
        WAITV(0);
        asm volatile("; TIE %0" : "+v"(gq));
        STAMP(7); CS(14);
        p2_join(acc2, w, lane, lds0);
        const float omg = rbf(1.0f - gate());
#pragma unroll
        for (int n = 0; n < 5; ++n) {
            const u32x4 v = {pack2(omg * rbf(acc2[0][n][0]), omg * rbf(acc2[0][n][1])), tag0, pack2(omg * rbf(acc2[0][n][2]), omg * rbf(acc2[0][n][3])), tag1};
            __builtin_amdgcn_raw_buffer_store_b128(v, vr, voff + n * 1024, 0, 16);
        }
        CS(15);
    } else {
        u32x2 xv[5], dv[5];
        p2_loop<false, 6>(p, acc2, src, ti, tj, w, lane, lds0, [&]() {
            gq = gload_sc1((unsigned)t * 16u, p.ws + WS_GATE);
            const unsigned dof = (unsigned)d0 * 2u;
#pragma unroll
            for (int n = 0; n < 5; ++n) asm volatile("global_load_dwordx2 %0, %1, %2 offset:%3" : "=v"(dv[n]) : "v"(dof), "s"(p.dsk), "n"(32 * n) : "memory");
        }, tag CSPASS);
        STAMP(7); CS(14);
        WAITV(0);
        asm volatile("; TIE %0 %1 %2 %3 %4 %5" : "+v"(dv[0]), "+v"(dv[1]), "+v"(dv[2]), "+v"(dv[3]), "+v"(dv[4]), "+v"(gq));
        u32x4 vv[5];
#pragma unroll
        for (int n = 0; n < 5; ++n) vv[n] = __builtin_amdgcn_raw_buffer_load_b128(vr, voff + n * 1024, 0, 16);
#pragma unroll
        for (int n = 0; n < 5; ++n) xv[n] = *(const LAS u32x2*)(size_t)(lds0 + 3 * S2 + (t - 128 * ti) * 160 + 32 * n + 8 * q);
        p2_join(acc2, w, lane, lds0);
        const float g = gate();
        f32x4 u4[5];
#pragma unroll
        for (int n = 0; n < 5; ++n)
#pragma unroll
            for (int e = 0; e < 4; ++e) {
                const unsigned xw = xv[n][e >> 1], dw = dv[n][e >> 1];
                u4[n][e] = g * (acc2[0][n][e] + rbf(((e & 1) ? bfhi(xw) : bflo(xw)) * ((e & 1) ? bfhi(dw) : bflo(dw))));
            }
        CS(15);
        for (unsigned spins = 0;; ++spins) {
            bool ok = true;
#pragma unroll
            for (int n = 0; n < 5; ++n) ok &= (vv[n][1] == tag0) & (vv[n][3] == tag1);
            if (__all(ok) || spins > SPIN_CAP) break;
            for (;; ++spins) {
                asm volatile("" ::: "memory");
                const u32x4 pr = __builtin_amdgcn_raw_buffer_load_b128(vr, vbase + 4096u, 0, 16);
                if (__all(pr[1] == tag0 && pr[3] == tag1) || spins > SPIN_CAP) break;
                __builtin_amdgcn_s_sleep(1);
            }
            asm volatile("" ::: "memory");
#pragma unroll
            for (int n = 0; n < 5; ++n) vv[n] = __builtin_amdgcn_raw_buffer_load_b128(vr, voff + n * 1024, 0, 16);
        }
        CS(16);
#pragma unroll
        for (int n = 0; n < 5; ++n) {
            f32x4 o4;
#pragma unroll
            for (int e = 0; e < 4; ++e) { const unsigned vw = vv[n][e & 2]; o4[e] = u4[n][e] + ((e & 1) ? bfhi(vw) : bflo(vw)); }
            *(f32x4*)(p.out + t * D + d0 + 16 * n) = o4;
        }
    }
    if (b == 0 && w == 7) {
        for (unsigned spins = 0;; ++spins) {
            bool ok = true;
#pragma unroll
            for (int k = 0; k < 7; ++k) {
                const int idx = 64 * k + lane, pn = idx >> 5, sl = idx & 31;
                const bool used = pn < 13 ? (sl < 14 || (sl >= 16 && sl < 21)) : sl < 9;
                ok &= ld_flag(p.ws, WS_FLAG + (used ? idx : 0) * 8) == tag;
            }
            if (__all(ok) || spins > SPIN_CAP) break;
            __builtin_amdgcn_s_sleep(1);
        }
        if (lane == 0) __hip_atomic_store((GAS unsigned long long*)(p.ws + WS_EPOCH), tag, RLX);
    }
    STAMP(8); CS(17);
}

void make_jobs(Params& p) {
    int role[8][32], pan[8][32], ct[8][32], cnt[8] = {0, 0, 0, 0, 0, 0, 0, 0};
    auto add = [&](int x, int r, int pn, int c) { const int k = cnt[x]++; role[x][k] = r; pan[x][k] = pn; ct[x][k] = c; };
    for (int x = 0; x < 4; ++x) {
        for (int pn = 3 * x; pn < 3 * x + 3; ++pn) for (int c = 0; c < 5; ++c) add(x, 1, pn, c);
        add(x, 1, 12, x);
        for (int pn = 3 * x; pn < 3 * x + 3; ++pn) for (int c = 0; c < 5; ++c) add(x, 0, pn, c);
    }
    add(4, 1, 12, 4);
    {
        int x = 4;
        auto put = [&](int pn, int c) { if (cnt[x] == 31) ++x; add(x, 0, pn, c); };
        for (int pn = 0; pn < 13; ++pn) for (int c = 5; c < 10; ++c) put(pn, c);
        for (int c = 0; c < 5; ++c) put(12, c);
        for (int pn = 12; pn >= 0; --pn) for (int c = 10; c < 14; ++c) put(pn, c);
    }
    for (int x = 0; x < 8; ++x) {
        while (cnt[x] < 31) add(x, 3, 0, 0);
        add(x, 2, x, 0);
        int isout[32], nout = 0;
        for (int r = 0; r < 32; ++r) { isout[r] = role[x][r] == 1; nout += isout[r]; }
        for (int r = 0; r < 32 && nout < 16; ++r) if (role[x][r] == 0) { isout[r] = 1; ++nout; }
        int no = 0, nd = 0;
        for (int r = 0; r < 32; ++r) {
            const int k = isout[r] ? no++ : nd++;
            const int ti = 4 * (x >> 1) + (k >> 2), tj = 4 * (x & 1) + (k & 3), b = 8 * r + x;
            p.job[b] = (unsigned)(role[x][r] | (pan[x][r] << 2) | (ct[x][r] << 6) | ((isout[r] ? 0 : 1) << 10) | ((ti * 8 + tj) << 11));
        }
    }
}

}

extern "C" void kernel_launch(void* const* d_in, const int* in_sizes, int n_in, void* d_out, int out_size, void* d_ws, size_t ws_size,
                              hipStream_t stream) {
    Params p;
    p.x = (const u16*)d_in[0]; p.lre = (const u16*)d_in[1]; p.lim = (const u16*)d_in[2];
    p.bre = (const u16*)d_in[3]; p.bim = (const u16*)d_in[4]; p.cre = (const u16*)d_in[5]; p.cim = (const u16*)d_in[6];
    p.logdt = (const u16*)d_in[7]; p.dsk = (const u16*)d_in[8];
    p.wg = (const u16*)d_in[10]; p.wu = (const u16*)d_in[11]; p.wd = (const u16*)d_in[12]; p.gw = (const u16*)d_in[13];
    p.out = (float*)d_out; p.ws = (char*)d_ws;
    make_jobs(p);
    hipLaunchKernelGGL(s5_swiglu_mix_kernel, dim3(256), dim3(512), 0, stream, p);
}
```

```cpp
#include <hip/hip_runtime.h>
#include <stdint.h>

namespace {

typedef unsigned short u16;
typedef __bf16 bf16x8 __attribute__((ext_vector_type(8)));
typedef __bf16 bf16x2 __attribute__((ext_vector_type(2)));
typedef short s16x4 __attribute__((ext_vector_type(4)));
typedef short s16x8 __attribute__((ext_vector_type(8)));
typedef float f32x2 __attribute__((ext_vector_type(2)));
typedef float f32x4 __attribute__((ext_vector_type(4)));
typedef unsigned u32x2 __attribute__((ext_vector_type(2)));
typedef unsigned u32x4 __attribute__((ext_vector_type(4)));
#define LAS __attribute__((address_space(3)))
#define GAS __attribute__((address_space(1)))
#define DI __device__ __forceinline__

constexpr int L = 2048, D = 640;
constexpr int PM = 160, NPAN = 13;
constexpr int KS2 = 94, KSF = 54;
constexpr unsigned WS_EPOCH = 0;
constexpr unsigned WS_FLAG = 1024;
constexpr unsigned WS_TFLAG = 4864;
constexpr unsigned WS_GRAN = 16384;
constexpr unsigned WS_GATE = 147456;
constexpr unsigned WS_VFLAG = 180224;
constexpr unsigned WS_A2 = 262144;
constexpr unsigned A2_BYTES = 128u * KS2 * 1024u;
constexpr unsigned WS_V = WS_A2 + A2_BYTES;
constexpr unsigned V_BYTES = 128u * 24u * 1024u;
constexpr unsigned WS_WT = WS_V + V_BYTES;
constexpr unsigned WT_BYTES = 8u * 14u * 20480u;
constexpr unsigned XS = 20480, STG = 53248;
constexpr unsigned LDS_MISC = 159744;
constexpr unsigned LDS_BYTES = 163840;
constexpr unsigned S2 = 20480, NSLOT = 7;
constexpr unsigned TR_STRIDE = 168, TR_WAVE = 64 * TR_STRIDE;
constexpr unsigned LDS_TR = 3 * S2, LDS_E = LDS_TR + 8 * TR_WAVE;
constexpr unsigned LDS_JUNK = LDS_MISC + 2560;
constexpr unsigned LDS_TAB = LDS_MISC + 64;
static_assert(LDS_E + 8192 <= LDS_MISC && LDS_TAB + 2048 <= 163840 && NSLOT * S2 <= LDS_MISC, "LDS map");
constexpr unsigned SPIN_CAP = 1u << 18;

struct Params {
    const u16 *x, *lre, *lim, *bre, *bim, *cre, *cim, *logdt, *dsk, *wg, *wu, *wd, *gw;
    float* out;
    char* ws;
};

typedef const __attribute__((address_space(4))) Params* KArgs;
DI KArgs late_args() { KArgs k = (KArgs)__builtin_amdgcn_kernarg_segment_ptr(); asm volatile("" : "+s"(k)); return k; }

#define WAITV(n) asm volatile("s_waitcnt vmcnt(" #n ")" ::: "memory")
#define WAITL0() asm volatile("s_waitcnt lgkmcnt(0)" ::: "memory")
#define BAR() do { asm volatile("" ::: "memory"); __builtin_amdgcn_s_barrier(); asm volatile("" ::: "memory"); } while (0)
#define SCHED() __builtin_amdgcn_sched_barrier(0)

DI void glds16(unsigned voff, const void* sbase, unsigned ldsdst) {
    asm volatile("s_mov_b32 m0, %2\n\ts_nop 0\n\tglobal_load_lds_dwordx4 %0, %1" ::"v"(voff), "s"(sbase), "s"(ldsdst) : "memory");
}
DI u32x4 gload_sc1(unsigned voff, const void* sbase) {
    u32x4 r;
    asm volatile("global_load_dwordx4 %0, %1, %2 sc1" : "=v"(r) : "v"(voff), "s"(sbase) : "memory");
    return r;
}
DI float bflo(unsigned v) { return __uint_as_float(v << 16); }
DI float bfhi(unsigned v) { return __uint_as_float(v & 0xffff0000u); }
DI unsigned pack2(float a, float b) { bf16x2 v = {(__bf16)a, (__bf16)b}; return __builtin_bit_cast(unsigned, v); }
DI float rbf(float a) { return bflo(pack2(a, a)); }
DI bf16x8 ldsr128(unsigned a) { return *(const LAS bf16x8*)(size_t)a; }
DI bf16x8 ldstr(unsigned a0, unsigned a1) {
    s16x4 lo = __builtin_amdgcn_ds_read_tr16_b64_v4i16((LAS s16x4*)(size_t)a0);
    s16x4 hi = __builtin_amdgcn_ds_read_tr16_b64_v4i16((LAS s16x4*)(size_t)a1);
    s16x8 v = __builtin_shufflevector(lo, hi, 0, 1, 2, 3, 4, 5, 6, 7);
    return __builtin_bit_cast(bf16x8, v);
}
template <int N> struct IC { static constexpr int value = N; };
#define RLX __ATOMIC_RELAXED, __HIP_MEMORY_SCOPE_AGENT

DI void horner2(float& ar, float& ai, float& br, float& bi, float lar, float lai, float nlai, float lbr, float lbi, float nlbi,
                float a0r, float a0i, float b0r, float b0i, float a1r, float a1i, float b1r, float b1i) {
    float t0, t1, t2, t3;
    asm("v_fma_f32 %4, %8, %0, %14\n\tv_fma_f32 %5, %8, %1, %15\n\tv_fma_f32 %6, %11, %2, %16\n\tv_fma_f32 %7, %11, %3, %17\n\t"
        "v_fmac_f32 %4, %10, %1\n\tv_fmac_f32 %5, %9, %0\n\tv_fmac_f32 %6, %13, %3\n\tv_fmac_f32 %7, %12, %2\n\t"
        "v_fma_f32 %0, %8, %4, %18\n\tv_fma_f32 %1, %8, %5, %19\n\tv_fma_f32 %2, %11, %6, %20\n\tv_fma_f32 %3, %11, %7, %21\n\t"
        "v_fmac_f32 %0, %10, %5\n\tv_fmac_f32 %1, %9, %4\n\tv_fmac_f32 %2, %13, %7\n\tv_fmac_f32 %3, %12, %6"
        : "+v"(ar), "+v"(ai), "+v"(br), "+v"(bi), "=&v"(t0), "=&v"(t1), "=&v"(t2), "=&v"(t3)
        : "v"(lar), "v"(lai), "v"(nlai), "v"(lbr), "v"(lbi), "v"(nlbi), "v"(a0r), "v"(a0i), "v"(b0r), "v"(b0i), "v"(a1r), "v"(a1i), "v"(b1r), "v"(b1i));
}
DI void cscale4(f32x4& re, f32x4& im, float cr, float ci, float nci) {
    float r0, r1, r2, r3, i0, i1, i2, i3;
    asm("v_mul_f32 %0, %8, %11\n\tv_mul_f32 %1, %8, %12\n\tv_mul_f32 %2, %8, %13\n\tv_mul_f32 %3, %8, %14\n\t"
        "v_mul_f32 %4, %8, %15\n\tv_mul_f32 %5, %8, %16\n\tv_mul_f32 %6, %8, %17\n\tv_mul_f32 %7, %8, %18\n\t"
        "v_fmac_f32 %0, %10, %15\n\tv_fmac_f32 %1, %10, %16\n\tv_fmac_f32 %2, %10, %17\n\tv_fmac_f32 %3, %10, %18\n\t"
        "v_fmac_f32 %4, %9, %11\n\tv_fmac_f32 %5, %9, %12\n\tv_fmac_f32 %6, %9, %13\n\tv_fmac_f32 %7, %9, %14"
        : "=&v"(r0), "=&v"(r1), "=&v"(r2), "=&v"(r3), "=&v"(i0), "=&v"(i1), "=&v"(i2), "=&v"(i3)
        : "v"(cr), "v"(ci), "v"(nci), "v"(re[0]), "v"(re[1]), "v"(re[2]), "v"(re[3]), "v"(im[0]), "v"(im[1]), "v"(im[2]), "v"(im[3]));
    re = (f32x4){r0, r1, r2, r3}; im = (f32x4){i0, i1, i2, i3};
}
DI void scan4(float& hr, float& hi, const f32x4& ur, const f32x4& ui, float lr, float li, float nli, u32x2& ore, u32x2& oim) {
    float r0 = ur[0], r1 = ur[1], r2 = ur[2], r3 = ur[3], i0 = ui[0], i1 = ui[1], i2 = ui[2], i3 = ui[3];
    unsigned a, b, c, d;
    asm("v_fmac_f32 %0, %12, %15\n\tv_fmac_f32 %4, %12, %16\n\tv_fmac_f32 %0, %14, %16\n\tv_fmac_f32 %4, %13, %15\n\t"
        "v_fmac_f32 %1, %12, %0\n\tv_fmac_f32 %5, %12, %4\n\tv_fmac_f32 %1, %14, %4\n\tv_fmac_f32 %5, %13, %0\n\t"
        "v_fmac_f32 %2, %12, %1\n\tv_fmac_f32 %6, %12, %5\n\tv_fmac_f32 %2, %14, %5\n\tv_fmac_f32 %6, %13, %1\n\t"
        "v_fmac_f32 %3, %12, %2\n\tv_fmac_f32 %7, %12, %6\n\tv_fmac_f32 %3, %14, %6\n\tv_fmac_f32 %7, %13, %2\n\t"
        "v_cvt_pk_bf16_f32 %8, %0, %1\n\tv_cvt_pk_bf16_f32 %9, %2, %3\n\tv_cvt_pk_bf16_f32 %10, -%4, -%5\n\tv_cvt_pk_bf16_f32 %11, -%6, -%7"
        : "+v"(r0), "+v"(r1), "+v"(r2), "+v"(r3), "+v"(i0), "+v"(i1), "+v"(i2), "+v"(i3), "=&v"(a), "=&v"(b), "=&v"(c), "=&v"(d)
        : "v"(lr), "v"(li), "v"(nli), "v"(hr), "v"(hi));
    hr = r3; hi = i3; ore = (u32x2){a, b}; oim = (u32x2){c, d};
}
DI unsigned gload_u16(unsigned voff, const void* sbase) { unsigned r; asm volatile("global_load_ushort %0, %1, %2" : "=v"(r) : "v"(voff), "s"(sbase) : "memory"); return r; }
DI void cmul(float ar, float ai, float br, float bi, float& cr, float& ci) { cr = ar * br - ai * bi; ci = ar * bi + ai * br; }

DI void ssm_params(unsigned raw_dt, unsigned raw_re, unsigned raw_im, float& lr, float& li, float& cr, float& ci) {
    const float dt = rbf(expf(bflo(raw_dt)));
    const float ar = bflo(raw_re), ai = bflo(raw_im);
    const float e = expf(dt * ar);
    float sn, cs;
    sincosf(dt * ai, &sn, &cs);
    lr = e * cs; li = e * sn;
    const float nr = lr - 1.0f, ni = li, den = ar * ar + ai * ai;
    cr = 2.0f * ((nr * ar + ni * ai) / den);
    ci = 2.0f * ((ni * ar - nr * ai) / den);
}
DI void pow20(float lr, float li, float& r, float& i) {
    float a2r, a2i, a4r, a4i, a5r, a5i, a10r, a10i;
    cmul(lr, li, lr, li, a2r, a2i); cmul(a2r, a2i, a2r, a2i, a4r, a4i); cmul(a4r, a4i, lr, li, a5r, a5i);
    cmul(a5r, a5i, a5r, a5i, a10r, a10i); cmul(a10r, a10i, a10r, a10i, r, i);
}

DI void mfma_acc(f32x4& c, const bf16x8& a, const bf16x8& b) { asm volatile("v_mfma_f32_16x16x32_bf16 %0, %1, %2, %0" : "+v"(c) : "v"(a), "v"(b)); }
DI void set_m0(unsigned a, unsigned b) { asm volatile("s_add_i32 m0, %0, %1" ::"s"(a), "s"(b) : "memory", "scc"); }
DI void glds_m0(unsigned voff, const void* sbase) { asm volatile("global_load_lds_dwordx4 %0, %1" ::"v"(voff), "s"(sbase) : "memory"); }
template <int OFF> DI void glds_m0_sc1(unsigned voff, const void* sbase) {
    asm volatile("global_load_lds_dwordx4 %0, %1 offset:%2 sc1" ::"v"(voff), "s"(sbase), "n"(OFF) : "memory");
}
template <int OFF> DI void glds_m0_half_sc1(unsigned voff, const void* sbase) {
    asm volatile("s_mov_b32 exec_hi, 0\n\tglobal_load_lds_dwordx4 %0, %1 offset:%2 sc1\n\ts_mov_b32 exec_hi, -1" ::"v"(voff), "s"(sbase), "n"(OFF) : "memory");
}
DI void glds_m0_half(unsigned voff, const void* sbase) {
    asm volatile("s_mov_b32 exec_hi, 0\n\tglobal_load_lds_dwordx4 %0, %1\n\ts_mov_b32 exec_hi, -1" ::"v"(voff), "s"(sbase) : "memory");
}
template <int N> DI void waitv() { asm volatile("s_waitcnt vmcnt(%0)" ::"n"(N) : "memory"); }
template <int N> DI unsigned smul(unsigned a) { unsigned r; asm("s_mul_i32 %0, %1, %2" : "=s"(r) : "s"(a), "n"(N)); return r; }
#define LGKM0() __builtin_amdgcn_s_waitcnt(0xC07F)
DI unsigned opaque(unsigned v) { asm volatile("" : "+v"(v)); return v; }

DI void wt_request(const Params& p, int u, int w, int lane, unsigned buf) {
    const int s = u / 5, cc = u - 5 * s, row = 4 * w + (lane >> 4);
    glds16((unsigned)(32 * s + row) * 1280u + (unsigned)(256 * cc) + (unsigned)(((lane & 15) ^ ((row & 7) << 1)) * 16), p.wd, buf + w * 1024);
}
DI bf16x8 wt_read(int w, int lane, unsigned buf) {
    const int row = lane >> 2, pp = lane & 3;
    const unsigned a = buf + row * 256 + (((2 * w + (pp >> 1)) ^ ((row & 7) << 1)) * 16) + 8 * (pp & 1);
    return ldstr(a, a + 4096);
}
DI void wt_store(const Params& p, int u, int w, int lane, const bf16x8& v) {
    const int s = u / 5, cc = u - 5 * s, d16 = 8 * cc + w, tj = d16 / 5, n = d16 - 5 * tj;
    const __amdgpu_buffer_rsrc_t r = __builtin_amdgcn_make_buffer_rsrc(p.ws + WS_WT, 0, WT_BYTES, 0x00020000);
    __builtin_amdgcn_raw_buffer_store_b128(__builtin_bit_cast(u32x4, v), r, (unsigned)((tj * 14 + (s >> 2)) * 20 + (s & 3) * 5 + n) * 1024u + lane * 16, 0, 16);
}
DI unsigned wt_flag(int u) { return WS_TFLAG + ((u % 5) * 64 + u / 5) * 8; }

template <bool SSM, int TH, class HF, class H0, class H, class H2, class H3, class H4>
DI void p1_main(const Params& p, f32x4 (&acc)[5][4], int pan, int ct, int w, int lane, unsigned lds0, HF first, H0 gap, H pre, H2 warm, H3 late, H4 fin) {
    first();
    constexpr int th = TH;
    const int c = w & 3, t0 = pan * PM;
    constexpr int NX = TH ? 2 : 3, NP = NX + 4;
    unsigned xoff[3], woff[4];
#pragma unroll
    for (int i = 0; i < NX; ++i) {
        const int row = 8 * (w + 8 * i) + (lane >> 3);
        int tr = row;
        if (SSM) { const int hh = row >= 80 ? 1 : 0, rr = row - 80 * hh, b = rr >> 4, rho = rr & 15; tr = 80 * hh + 20 * (rho >> 2) + 4 * b + (rho & 3); }
        const int g = min(t0 + tr, L - 1);
        xoff[i] = (unsigned)g * 1280u + (unsigned)(((lane & 7) ^ ((row >> 1) & 7)) * 16);
    }
    const char* wb0; const char* wb1; unsigned wstep;
    if (!SSM) {
        const int kq = lane >> 4, swz = kq | (((w >> 2) & 1) << 2);
        int j = (lane & 15) ^ (swz << 1);
        if (ct == 13) j &= 7;
#pragma unroll
        for (int i = 0; i < 4; ++i) woff[i] = (unsigned)(4 * (w >> 1) + 16 * i + kq) * 3456u + (unsigned)(128 * ct + 8 * j) * 2u;
        wb0 = wb1 = (const char*)((w & 1) ? p.wu : p.wg);
        wstep = 64u * 3456u;
    } else {
        const int r = 8 * w + (lane >> 3);
#pragma unroll
        for (int i = 0; i < 4; ++i) woff[i] = (unsigned)(128 * ct + r + 64 * (i & 1)) * 1280u + (unsigned)(((lane & 7) ^ ((r >> 1) & 7)) * 16);
        wb0 = (const char*)p.bre; wb1 = (const char*)p.bim;
        wstep = 128u;
    }
    const char* xb = (const char*)p.x;
    const unsigned mX = lds0 + w * 1024, mW = lds0 + XS + w * 1024;
    auto stW = [&](unsigned slot, int i) { set_m0(slot, mW + i * 8192); asm volatile("s_nop 0"); glds_m0(woff[i], (SSM && i >= 2) ? wb1 : wb0); };
    auto stX = [&](unsigned slot, int i) { set_m0(slot, mX + i * 8192); asm volatile("s_nop 0"); glds_m0(xoff[i], xb); };
    const int rho = lane & 15, q = lane >> 4;
    const unsigned xa0 = lds0 + (unsigned)(80 * th + rho) * 128u + (unsigned)((q ^ ((rho >> 1) & 7)) * 16), xa1 = xa0 ^ 64u;
    unsigned wa0, wa1;
    if (!SSM) {
        const int qq = (lane & 15) >> 2, pp = lane & 3, x = (qq << 1) | ((q & 1) << 3);
        wa0 = lds0 + XS + q * 4096 + qq * 256 + (((4 * c + (pp >> 1)) ^ x) * 16) + 8 * (pp & 1);
        wa1 = wa0 ^ 32u;
    } else {
        wa0 = lds0 + XS + (unsigned)(32 * c + rho) * 128u + (unsigned)((q ^ ((rho >> 1) & 7)) * 16);
        wa1 = wa0 ^ 64u;
    }
    bf16x8 xf[2][5], wf[2][4];
    unsigned bx, bw0, bw1;
    auto bases = [&](unsigned slot, int s) {
        bx = opaque(slot + (s ? xa1 : xa0));
        if (!SSM) { bw0 = opaque(slot + wa0); bw1 = opaque(slot + wa1); } else bw0 = opaque(slot + (s ? wa1 : wa0));
    };
    auto rdW = [&](int set, int s, int i) {
        if (!SSM) { const unsigned a = ((i >> 1) ? bw1 : bw0) + s * 16384 + (i & 1) * 1024; wf[set][i] = ldstr(a, a + 2048); }
        else wf[set][i] = ldsr128(bw0 + (i >> 1) * 16384 + (i & 1) * 2048);
    };
    auto rdX = [&](int set, int b) { xf[set][b] = ldsr128(bx + b * 2048); };
    auto mm = [&](int set, int i) {
        if (SSM) mfma_acc(acc[i >> 2][i & 3], xf[set][i >> 2], wf[set][i & 3]); else mfma_acc(acc[i >> 2][i & 3], wf[set][i & 3], xf[set][i >> 2]);
    };
    auto stage = [&](unsigned slot) {
#pragma unroll
        for (int i = 0; i < 4; ++i) stW(slot, i);
#pragma unroll
        for (int i = 0; i < NX; ++i) stX(slot, i);
        wb0 += wstep; wb1 += wstep; xb += 128;
    };
    stage(0);
    SCHED();
    gap();
#pragma unroll
    for (int b = 0; b < 5; ++b)
#pragma unroll
        for (int blk = 0; blk < 4; ++blk) acc[b][blk] = (f32x4){0.f, 0.f, 0.f, 0.f};
    asm volatile("" : "+v"(acc[0][0]), "+v"(acc[0][1]), "+v"(acc[0][2]), "+v"(acc[0][3]), "+v"(acc[1][0]), "+v"(acc[1][1]), "+v"(acc[1][2]), "+v"(acc[1][3]),
                      "+v"(acc[2][0]), "+v"(acc[2][1]), "+v"(acc[2][2]), "+v"(acc[2][3]), "+v"(acc[3][0]), "+v"(acc[3][1]), "+v"(acc[3][2]), "+v"(acc[3][3]),
                      "+v"(acc[4][0]), "+v"(acc[4][1]), "+v"(acc[4][2]), "+v"(acc[4][3]));
    SCHED();
    stage(STG);
    waitv<2 * NP>();
    pre();
    waitv<NP>();
    BAR();
    if (TH == 1) __builtin_amdgcn_s_setprio(1);
    bases(0, 0);
#pragma unroll
    for (int i = 0; i < 4; ++i) rdW(0, 0, i);
#pragma unroll
    for (int i = 0; i < 5; ++i) rdX(0, i);
    bases(0, 1);
    if (TH == 1) stage(2 * STG);
    unsigned cur = 0, nx1 = STG, nx2 = 2 * STG;
    auto body = [&](auto kind, int t) {
        constexpr int K = decltype(kind)::value;
        auto piece = [&](int i, unsigned slot) {
            if (i >= 5 && i < 19) {
                const int k = (i - 5) >> 1;
                if (((i - 5) & 1) == 0) set_m0(slot, k < 4 ? mW + k * 8192 : mX + (k - 4) * 8192);
                else if (k < 4) glds_m0(woff[k], (SSM && k >= 2) ? wb1 : wb0);
                else glds_m0(xoff[k - 4], xb);
            }
        };
        if (K == 3) late();
        LGKM0(); SCHED();
#pragma unroll
        for (int i = 0; i < 20; ++i) {
            mm(0, i);
            if (i < 4) rdW(1, 1, i);
            else if (i < 9) rdX(1, i - 4);
            if (TH == 0 && K == -1) piece(i, nx2);
            if (K < 3 && i == 19) bases(nx1, 0);
            SCHED();
        }
        if (TH == 0 && K == -1) { wb0 += wstep; wb1 += wstep; xb += 128; }
        if (K < 2) waitv<NP>(); else if (K == 2) waitv<TH>();
        LGKM0(); BAR(); SCHED();
        if (TH == 1 && K == 0) { stage(cur); SCHED(); }
        if (TH == 1 && K == 1) { warm(); SCHED(); }
#pragma unroll
        for (int i = 0; i < 20; ++i) {
            mm(1, i);
            if (K < 3) {
                if (i < 4) rdW(0, 0, i);
                else if (i < 9) rdX(0, i - 4);
                if (TH == 0 && K <= 0) piece(i, cur);
                if (i == 19) bases(nx1, 1);
            }
            SCHED();
        }
        if (TH == 0 && K <= 0) { wb0 += wstep; wb1 += wstep; xb += 128; }
        const unsigned o = cur; cur = nx1; nx1 = nx2; nx2 = o;
    };
    if (TH == 0) body(IC<-1>{}, 0);
#pragma unroll 1
    for (int t = TH == 0 ? 1 : 0; t < 7; ++t) body(IC<0>{}, t);
    body(IC<1>{}, 7); body(IC<2>{}, 8); body(IC<3>{}, 9);
    if (TH == 1) __builtin_amdgcn_s_setprio(0);
    asm volatile("s_nop 7\n\ts_nop 7"
                 : "+v"(acc[0][0]), "+v"(acc[0][1]), "+v"(acc[0][2]), "+v"(acc[0][3]), "+v"(acc[1][0]), "+v"(acc[1][1]), "+v"(acc[1][2]), "+v"(acc[1][3]),
                   "+v"(acc[2][0]), "+v"(acc[2][1]), "+v"(acc[2][2]), "+v"(acc[2][3]), "+v"(acc[3][0]), "+v"(acc[3][1]), "+v"(acc[3][2]), "+v"(acc[3][3]),
                   "+v"(acc[4][0]), "+v"(acc[4][1]), "+v"(acc[4][2]), "+v"(acc[4][3])::"memory");
    fin();
}

constexpr int p2_pos(int k, int e) { return k <= 1 ? -1 : k == 2 ? 0 : k == 3 ? 1 : 2 * (k - 3) + e; }
constexpr int p2_npieces(bool down) { return down ? 21 : 12; }
constexpr int p2_piece_step(int g) { return g >> 1; }
constexpr int p2_after(bool down, int NX, int kind, int a, int b, int T, int point) {
    const int NT = down ? 14 : 10;
    int n = 0; bool seen = false;
    for (int k = 0; k < 2; ++k) for (int h = 0; h < 2; ++h) { if (seen) n += 2; if (kind == 0 && a == k && b == h) seen = true; }
    for (int t = 0; t < NT; ++t) {
        if (t == (down ? NT - 2 : NT - 1) && seen) n += NX;
        if (t == T && point == 0) return n;
        if (t == T && point == 1) return n;
        if (down && t == 0) { if (seen) n += 1; if (kind == 2) seen = true; }
        for (int k = 2; k < NT; ++k) for (int h = 0; h < 2; ++h)
            if (p2_pos(k, h) == 2 * t) { if (seen) n += 2; if (kind == 0 && a == k && b == h) seen = true; }
        if (t == T && point == 2) return n;
        for (int g = 0; g < p2_npieces(down); ++g)
            if (p2_piece_step(g) == t) { if (seen) n += 1; if (kind == 1 && a == g) seen = true; }
        for (int k = 2; k < NT; ++k) for (int h = 0; h < 2; ++h)
            if (p2_pos(k, h) == 2 * t + 1) { if (seen) n += 2; if (kind == 0 && a == k && b == h) seen = true; }
    }
    return n;
}
constexpr int p2_min(int a, int b) { return a < b ? a : b; }
constexpr int p2_mid(bool down, int NX, int T) {
    const int NT = down ? 14 : 10, a = p2_after(down, NX, 0, T, 1, T, 2);
    return (T + 1 >= (int)NSLOT && T + 1 < NT) ? p2_min(a, p2_after(down, NX, 1, 3 * (T + 1 - (int)NSLOT) + 2, 0, T, 2)) : a;
}
static_assert(p2_after(true, 1, 0, 0, 0, 0, 0) == 6 && p2_after(true, 1, 0, 0, 1, 0, 2) == 9 && p2_after(true, 1, 2, 0, 0, 1, 1) == 10 && p2_mid(true, 1, 13) == 1 && p2_mid(true, 1, 12) == 3 && p2_mid(false, 7, 9) == 7, "vmcnt schedule");
struct W2Src { unsigned vo[3]; };
template <bool DOWN> DI W2Src w2_src(int tj, int w, int lane) {
    W2Src r;
#pragma unroll
    for (int i = 0; i < 3; ++i) {
        if (DOWN) r.vo[i] = (unsigned)(2560 * w + lane * 16);
        else {
            const int row = min(20 * w + 8 * i + (lane >> 3), 159), hi = row >= 80 ? 1 : 0;
            r.vo[i] = (unsigned)(80 * tj + row - 80 * hi) * 1280u + (unsigned)(128 * hi) + (unsigned)(((lane & 7) ^ ((row >> 1) & 7)) * 16);
        }
    }
    return r;
}
template <bool DOWN> DI void w2_piece(const Params& p, const W2Src& s, int T, int tj, int w, int i) {
    if (DOWN) {
        const char* base = p.ws + WS_WT + (unsigned)(tj * 14 + T) * 20480u;
        set_m0(smul<2560>(w), (unsigned)((T % NSLOT) * S2));
        asm volatile("s_nop 0");
        if (i == 0) glds_m0_sc1<0>(s.vo[0], base); else if (i == 1) glds_m0_sc1<1024>(s.vo[0], base); else glds_m0_half_sc1<2048>(s.vo[0], base);
    } else {
        const char* base = (const char*)(T >= 5 ? p.cim : p.cre) + (unsigned)(T >= 5 ? T - 5 : T) * 256u;
        set_m0(smul<2560>(w), (unsigned)((T % NSLOT) * S2 + i * 1024));
        asm volatile("s_nop 0");
        if (i < 2) glds_m0(s.vo[i], base); else glds_m0_half(s.vo[i], base);
    }
}
DI const char* c_warm_addr(const Params& p, int b, int w, int lane) {
    const char *cre = (const char*)p.cre, *cim = (const char*)p.cim;
    asm volatile("" : "+v"(lane), "+s"(cre), "+s"(cim));
    const int m = min(((b >> 3) * 4 + (w & 3)) * 50 + min(lane, 49), 6399), im = m >= 3200 ? 1 : 0, r = m - 3200 * im, row = r / 10;
    return (im ? cim : cre) + (320 * (b & 1) + row) * 1280 + 128 * (r - 10 * row);
}
DI void l2_warm(const char* a) { asm volatile("s_mov_b32 m0, %1\n\ts_nop 0\n\tglobal_load_lds_dword %0, off" ::"v"(a), "s"(LDS_JUNK) : "memory"); }
DI void x_piece(const Params& p, int ti, int tj, int w, int lane, int i) {
    asm volatile("" : "+v"(lane));
    const int idx = min(160 * w + 64 * i + lane, 1279), row = idx / 10, gr = idx - 10 * row;
    set_m0(smul<2560>(w), (unsigned)(3 * S2 + i * 1024));
    asm volatile("s_nop 0");
    const unsigned vo = (unsigned)(128 * ti + row) * 1280u + (unsigned)(80 * tj + 8 * gr) * 2u;
    if (i < 2) glds_m0(vo, p.x); else glds_m0_half(vo, p.x);
}
template <int N> DI void check_flags(u32x2& f, unsigned long long tag, unsigned voff, const char* base, unsigned poll) {
    unsigned cnt;
    asm volatile("s_waitcnt vmcnt(%5)\n\t"
                 "v_cmp_eq_u64 vcc, %0, %2\n\t"
                 "s_cmp_eq_u64 vcc, exec\n\t"
                 "s_cbranch_scc1 2f\n\t"
                 "s_cmp_eq_u32 %6, 0\n\t"
                 "s_cbranch_scc1 2f\n\t"
                 "s_mov_b32 %1, 0\n"
                 "1:\n\t"
                 "global_load_dwordx2 %0, %3, %4 sc1\n\t"
                 "s_waitcnt vmcnt(0)\n\t"
                 "v_cmp_eq_u64 vcc, %0, %2\n\t"
                 "s_cmp_eq_u64 vcc, exec\n\t"
                 "s_cbranch_scc1 2f\n\t"
                 "s_add_u32 %1, %1, 1\n\t"
                 "s_cmp_gt_u32 %1, 0x40000\n\t"
                 "s_cbranch_scc1 2f\n\t"
                 "s_sleep 1\n\t"
                 "s_branch 1b\n"
                 "2:"
                 : "+v"(f), "=&s"(cnt)
                 : "v"(tag), "v"(voff), "s"(base), "n"(N), "s"(poll)
                 : "vcc", "scc", "memory");
}
DI void warm_code(int b, int w, int lane) {
    unsigned long long pc; unsigned len;
    asm volatile("s_getpc_b64 %0\n.Lmix_code_here:\n\ts_mov_b32 %1, .Lmix_code_end-.Lmix_code_here" : "=s"(pc), "=s"(len));
    const unsigned off = min((unsigned)((((b >> 3) & 3) * 8 + w) * 16 + (lane & 15)) * 128u, len & ~127u);
    asm volatile("s_mov_b32 m0, %2\n\ts_nop 0\n\tglobal_load_lds_dword %0, %1" ::"v"(off), "s"(pc & ~127ull), "s"(LDS_JUNK) : "memory");
}
DI void warm_ctrl(const Params& p, int b, int lane) {
    const unsigned off = (unsigned)min(b * 6 + min(lane, 5), 1471) * 128u;
    asm volatile("s_mov_b32 m0, %2\n\ts_nop 0\n\tglobal_load_lds_dword %0, %1" ::"v"(off), "s"(p.ws), "s"(LDS_JUNK) : "memory");
}
DI void tie2(u32x4& a, u32x4& b) { asm volatile("; TIE %0 %1" : "+v"(a), "+v"(b)); }

template <bool DOWN, int NEXTRA, class H, class H2> DI void p2_loop(const Params& p, f32x4 (&acc)[2][5], const W2Src& src, int ti, int tj, int w, int lane, unsigned lds0, H extra, H2 take, unsigned long long tag) {
    constexpr int NT = DOWN ? 14 : 10, TX = DOWN ? NT - 2 : NT - 1;
    const int tg = w & 3, kh = w >> 2;
    const char* a2 = p.ws + WS_A2 + (unsigned)((DOWN ? 0 : KSF) + 2 * kh) * 1024u;
    unsigned va[2];
#pragma unroll
    for (int m = 0; m < 2; ++m) va[m] = (unsigned)((8 * ti + 2 * tg + (m ^ kh)) * KS2) * 1024u + lane * 16;
    u32x4 A[3][2][2];
    auto ldA = [&](int T, int e) {
#pragma unroll
        for (int m = 0; m < 2; ++m) A[T % 3][e][m] = gload_sc1(va[m], a2 + (4 * T + e) * 1024);
    };
    ldA(0, 0); ldA(0, 1);
    unsigned lb0, lb1;
    if (DOWN) { lb0 = lds0 + (unsigned)(10240 * kh + lane * 16); lb1 = lb0 + 5120; }
    else { const int rho = lane & 15, q = lane >> 4; lb0 = lds0 + (unsigned)(80 * kh + rho) * 128u + (unsigned)((q ^ ((rho >> 1) & 7)) * 16); lb1 = lb0 ^ 64u; }
    bf16x8 wf[2][5];
    unsigned rb;
    auto rdW = [&](int set, int n) { wf[set][n] = ldsr128(rb + n * (DOWN ? 1024 : 2048)); };
#pragma unroll
    for (int m = 0; m < 2; ++m)
#pragma unroll
        for (int n = 0; n < 5; ++n) acc[m][n] = (f32x4){0.f, 0.f, 0.f, 0.f};
    asm volatile("" : "+v"(acc[0][0]), "+v"(acc[0][1]), "+v"(acc[0][2]), "+v"(acc[0][3]), "+v"(acc[0][4]), "+v"(acc[1][0]), "+v"(acc[1][1]), "+v"(acc[1][2]),
                      "+v"(acc[1][3]), "+v"(acc[1][4]));
    rb = opaque(lb0);
#pragma unroll
    for (int n = 0; n < 5; ++n) rdW(0, n);
    SCHED();
    BAR();
    ldA(1, 0); ldA(1, 1);
    u32x2 lf;
    const int lk = lane & 15;
    const unsigned lfo = WS_FLAG + ((((lane >> 4) & 1) ? (128 * ti + 127) / PM : (128 * ti) / PM) * 32 + 5 + (lk < 9 ? lk : 0)) * 8;
    auto issue = [&](int pos) {
        if (DOWN && pos == 0) asm volatile("global_load_dwordx2 %0, %1, %2 sc1" : "=v"(lf) : "v"(lfo), "s"(p.ws) : "memory");
        if (DOWN && pos == 2) check_flags<p2_after(true, NEXTRA, 2, 0, 0, 1, 1)>(lf, tag, lfo, p.ws, __builtin_amdgcn_readfirstlane(w == 0 ? 1 : 0));
#pragma unroll
        for (int k = 2; k < NT; ++k)
#pragma unroll
            for (int h = 0; h < 2; ++h) if (p2_pos(k, h) == pos) ldA(k, h);
    };
    auto step = [&](auto tc) {
        constexpr int T = decltype(tc)::value;
        const bool live = !(DOWN && T == 13) || kh == 0;
        if (T == TX) extra();
        if (DOWN && T == 13) { waitv<0>(); tie2(A[T % 3][1][0], A[T % 3][1][1]); take(); }
        waitv<p2_after(DOWN, NEXTRA, 0, T, 0, T, 0)>(); tie2(A[T % 3][0][0], A[T % 3][0][1]);
        rb = opaque(lb1 + (T % NSLOT) * S2);
        LGKM0(); SCHED();
        if (live) {
#pragma unroll
            for (int i = 0; i < 10; ++i) {
                mfma_acc(acc[i / 5][i % 5], wf[0][i % 5], __builtin_bit_cast(bf16x8, A[T % 3][0][i / 5]));
                if (i < 5) rdW(1, i);
                SCHED();
            }
        }
        issue(2 * T);
        waitv<p2_mid(DOWN, NEXTRA, T)>(); tie2(A[T % 3][1][0], A[T % 3][1][1]);
        LGKM0(); BAR(); SCHED();
        if (T + 1 < NT) rb = opaque(lb0 + ((T + 1) % NSLOT) * S2);
        if (live) {
#pragma unroll
            for (int i = 0; i < 10; ++i) {
                mfma_acc(acc[i / 5][i % 5], wf[1][i % 5], __builtin_bit_cast(bf16x8, A[T % 3][1][i / 5]));
                if (T + 1 < NT && i < 5) rdW(0, i);
                if (i == 5 || i == 7) {
                    const int g = 2 * T + (i == 7 ? 1 : 0);
                    if (g < p2_npieces(DOWN)) { if (!DOWN && g >= 9) x_piece(p, ti, tj, w, lane, g - 9); else w2_piece<DOWN>(p, src, NSLOT + g / 3, tj, w, g % 3); }
                }
                SCHED();
            }
        }
        issue(2 * T + 1);
        SCHED();
    };
    step(IC<0>{}); step(IC<1>{}); step(IC<2>{}); step(IC<3>{}); step(IC<4>{}); step(IC<5>{}); step(IC<6>{}); step(IC<7>{}); step(IC<8>{}); step(IC<9>{});
    if (DOWN) { step(IC<10>{}); step(IC<11>{}); step(IC<12>{}); step(IC<13>{}); }
    asm volatile("s_nop 7\n\ts_nop 7"
                 : "+v"(acc[0][0]), "+v"(acc[0][1]), "+v"(acc[0][2]), "+v"(acc[0][3]), "+v"(acc[0][4]), "+v"(acc[1][0]), "+v"(acc[1][1]), "+v"(acc[1][2]),
                   "+v"(acc[1][3]), "+v"(acc[1][4])::"memory");
}
DI void p2_join(f32x4 (&acc)[2][5], int w, int lane, unsigned lds0) {
    const unsigned xw = lds0 + w * 5120 + lane * 16, xr = lds0 + (w ^ 4) * 5120 + lane * 16;
#pragma unroll
    for (int n = 0; n < 5; ++n) *(LAS f32x4*)(size_t)(xw + n * 1024) = acc[1][n];
    WAITL0(); BAR();
#pragma unroll
    for (int n = 0; n < 5; ++n) acc[0][n] += *(const LAS f32x4*)(size_t)(xr + n * 1024);
}

DI void gate_rows(const Params& p, int gi, int w, int lane, unsigned tag0, unsigned tag1) {
    const int i16 = lane & 15, rr = lane >> 4, row0 = 64 * gi + 8 * w;
    u32x4 gw[5], xv[2][5];
    const u16* gwp = late_args()->gw;
#pragma unroll
    for (int k = 0; k < 5; ++k) gw[k] = *(const u32x4*)(gwp + 8 * (i16 + 16 * k));
#pragma unroll
    for (int ps = 0; ps < 2; ++ps)
#pragma unroll
        for (int k = 0; k < 5; ++k) xv[ps][k] = *(const u32x4*)(p.x + (row0 + 4 * ps + rr) * D + 8 * (i16 + 16 * k));
    const __amdgpu_buffer_rsrc_t gr = __builtin_amdgcn_make_buffer_rsrc(p.ws + WS_GATE, 0, 2048 * 16, 0x00020000);
#pragma unroll
    for (int ps = 0; ps < 2; ++ps) {
        float s = 0.f;
#pragma unroll
        for (int k = 0; k < 5; ++k)
#pragma unroll
            for (int e = 0; e < 4; ++e) {
                const unsigned pk = pack2(bflo(xv[ps][k][e]) * bflo(gw[k][e]), bfhi(xv[ps][k][e]) * bfhi(gw[k][e]));
                s += bflo(pk); s += bfhi(pk);
            }
        s += __shfl_xor(s, 8); s += __shfl_xor(s, 4); s += __shfl_xor(s, 2); s += __shfl_xor(s, 1);
        const float z = rbf(s), g = rbf(1.0f / (1.0f + expf(-z)));
        __builtin_amdgcn_raw_buffer_store_b128((u32x4){__float_as_uint(g), tag0, 0u, tag1}, gr, i16 == 0 ? (unsigned)(row0 + 4 * ps + rr) * 16u : 0x7fffff00u, 0, 16);
    }
}
DI unsigned long long ld_flag(const char* ws, unsigned off) { return __hip_atomic_load((GAS unsigned long long*)(ws + off), RLX); }

DI void spin_flags(const char* ws, unsigned off, unsigned long long tag) {
    for (unsigned spins = 0;; ++spins) {
        if (__all(ld_flag(ws, off) == tag) || spins > SPIN_CAP) break;
        __builtin_amdgcn_s_sleep(1);
    }
    asm volatile("" ::: "memory");
}

struct JobTable { unsigned v[256]; };
constexpr JobTable make_jobs() {
    JobTable t = {};
    int role[8][32] = {}, pan[8][32] = {}, ct[8][32] = {}, cnt[8] = {};
    auto add = [&](int x, int r, int pn, int c) { const int k = cnt[x]++; role[x][k] = r; pan[x][k] = pn; ct[x][k] = c; };
    for (int x = 0; x < 4; ++x) {
        for (int pn = 3 * x; pn < 3 * x + 3; ++pn) for (int c = 0; c < 5; ++c) add(x, 1, pn, c);
        add(x, 1, 12, x);
        for (int pn = 3 * x; pn < 3 * x + 3; ++pn) for (int c = 0; c < 5; ++c) add(x, 0, pn, c);
    }
    add(4, 1, 12, 4);
    int x1 = 4;
    auto put = [&](int pn, int c) { if (cnt[x1] == 31) ++x1; add(x1, 0, pn, c); };
    for (int pn = 0; pn < 13; ++pn) for (int c = 5; c < 10; ++c) put(pn, c);
    for (int c = 0; c < 5; ++c) put(12, c);
    for (int pn = 12; pn >= 0; --pn) for (int c = 10; c < 14; ++c) put(pn, c);
    int ngate = 0;
    for (int x = 0; x < 8; ++x) {
        while (cnt[x] < 31) add(x, 3, 8, 0);
        add(x, 3, x, 0);
        int isout[32] = {}, nout = 0;
        for (int r = 0; r < 32; ++r) { isout[r] = role[x][r] == 1; nout += isout[r]; }
        for (int r = 0; r < 32 && nout < 16; ++r) if (role[x][r] == 0) { isout[r] = 1; ++nout; }
        int no = 0, nd = 0;
        for (int r = 0; r < 32; ++r) {
            const int k = isout[r] ? no++ : nd++;
            const int ti = 4 * (x >> 1) + (k >> 2), tj = 4 * (x & 1) + (k & 3);
            const int gi = (role[x][r] == 0 && isout[r] && ngate < 32) ? ngate++ : 63;
            t.v[8 * r + x] = (unsigned)(role[x][r] | (pan[x][r] << 2) | (ct[x][r] << 6) | ((isout[r] ? 0 : 1) << 10) | ((ti * 8 + tj) << 11) | (gi << 18));
        }
    }
    return t;
}
__device__ const JobTable kJobs = make_jobs();

__global__ __launch_bounds__(512, 2) void s5_swiglu_mix_kernel(Params p) {
    __shared__ __attribute__((aligned(1024))) char lds[LDS_BYTES];
    const int tid = threadIdx.x, lane = tid & 63;
    const int w = __builtin_amdgcn_readfirstlane(tid >> 6);
    const int b = blockIdx.x;
    const unsigned lds0 = (unsigned)(size_t)(LAS char*)lds;
    const unsigned job = kJobs.v[b];
    u32x2 e_in;
    const void *k0 = p.x, *k1 = p.ws;
    warm_code(b, w, lane);
    asm volatile("" ::"s"(k0), "s"(k1), "s"(job));
    if (w == 7) warm_ctrl(p, b, lane);
    auto ask_tag = [&]() { asm volatile("global_load_dwordx2 %0, %1, %2 sc1" : "=v"(e_in) : "v"(0u), "s"(p.ws + WS_EPOCH) : "memory"); };
    unsigned tag0 = 0, tag1 = 0;
    unsigned long long tag = 0;
    auto take_tag = [&]() { asm volatile("; TIE %0" : "+v"(e_in)); tag0 = e_in[0] + 1u; tag1 = e_in[1] + 1u; tag = ((unsigned long long)tag1 << 32) | tag0; };
    const __amdgpu_buffer_rsrc_t a2r = __builtin_amdgcn_make_buffer_rsrc(p.ws + WS_A2, 0, A2_BYTES, 0x00020000);

    const int role = job & 3;
    const int pan = (job >> 2) & 15, ct = (job >> 6) & 15;
    const bool down = (job >> 10) & 1;
    const int pair = (job >> 11) & 127, ti = pair >> 3, tj = pair & 7;
    const unsigned flag_off = WS_FLAG + (role == 0 ? pan * 32 + ct : role == 1 ? pan * 32 + 16 + ct : 13 * 32 + pan) * 8;
    W2Src src;
    auto make_src = [&]() { int l2 = lane; asm volatile("" : "+v"(l2)); src = down ? w2_src<true>(tj, w, l2) : w2_src<false>(tj, w, l2); };
    const char* warm_a;
    auto setup = [&]() { make_src(); warm_a = c_warm_addr(p, b, w, lane); };
    auto warm = [&]() { l2_warm(warm_a); };
    const unsigned tf0 = WS_TFLAG + (((5 * tj) >> 3) * 64 + min(lane, 53)) * 8, tf1 = WS_TFLAG + (((5 * tj + 4) >> 3) * 64 + min(lane, 53)) * 8;
    u32x2 tfa, tfb;
    auto tf_request = [&]() {
        asm volatile("global_load_dwordx2 %0, %2, %4 sc1\n\tglobal_load_dwordx2 %1, %3, %4 sc1" : "=&v"(tfa), "=&v"(tfb) : "v"(tf0), "v"(tf1), "s"(p.ws) : "memory");
    };
    bool tf_ok = false;
    auto tf_take = [&]() {
        asm volatile("; TIE %0 %1" : "+v"(tfa), "+v"(tfb));
        tf_ok = __all(tfa[0] == tag0 && tfa[1] == tag1 && tfb[0] == tag0 && tfb[1] == tag1);
    };

    if (role == 0) {
        f32x4 acc[5][4];
        auto fin = [&]() { WAITV(0); tf_take(); };
        if (w < 4) p1_main<false, 0>(p, acc, pan, ct, w, lane, lds0, ask_tag, setup, take_tag, warm, tf_request, fin);
        else p1_main<false, 1>(p, acc, pan, ct, w, lane, lds0, ask_tag, setup, take_tag, warm, tf_request, fin);
        if (down && !tf_ok) { spin_flags(p.ws, tf0, tag); spin_flags(p.ws, tf1, tag); }
        const int th = w >> 2, c = w & 3, s2 = 4 * ct + c;
        constexpr int NE1 = 6, NE2 = 6;
        auto epi = [&](auto dn) {
            constexpr bool DN = decltype(dn)::value != 0;
            auto w2_pc = [&](int k) { w2_piece<DN>(p, src, k / 3, tj, w, k % 3); };
#pragma unroll
            for (int bb = 0; bb < 5; ++bb) {
                float a[8];
#pragma unroll
                for (int k = 0; k < 8; ++k) a[k] = __builtin_amdgcn_exp2f(acc[bb][2 * (k >> 2)][k & 3] * -1.44269504088896f);
                if (2 * bb < NE1) w2_pc(2 * bb);
                SCHED();
#pragma unroll
                for (int k = 0; k < 8; ++k) a[k] = __builtin_amdgcn_rcpf(1.0f + a[k]);
                if (2 * bb + 1 < NE1) w2_pc(2 * bb + 1);
                SCHED();
#pragma unroll
                for (int k = 0; k < 8; ++k) a[k] = acc[bb][2 * (k >> 2)][k & 3] * a[k] * acc[bb][2 * (k >> 2) + 1][k & 3];
                const u32x4 v = {pack2(a[0], a[1]), pack2(a[2], a[3]), pack2(a[4], a[5]), pack2(a[6], a[7])};
                const int tb = 10 * pan + 5 * th + bb;
                if (tb < 128 && s2 < KSF) __builtin_amdgcn_raw_buffer_store_b128(v, a2r, (unsigned)(tb * KS2 + s2) * 1024u + lane * 16, 0, 16);
                SCHED();
            }
#pragma unroll
            for (int k = NE1; k < NE1 + NE2; ++k) w2_pc(k);
            waitv<NE2>(); BAR();
            if (w == 0) __hip_atomic_store((GAS unsigned long long*)(p.ws + flag_off), tag, RLX);
            if (!DN) {
                const int gi = (job >> 18) & 63;
                if (gi < 32) gate_rows(p, gi, w, lane, tag0, tag1);
            }
#pragma unroll
            for (int k = NE1 + NE2; k < 3 * (int)NSLOT; ++k) w2_pc(k);
        };
        if (down) epi(IC<1>{}); else epi(IC<0>{});
    } else if (role == 1) {
        const int th = w >> 2, c = w & 3, sg = lane & 15, q = lane >> 4;
        float t20r = 0.f, t20i = 0.f, t160r = 0.f, t160i = 0.f;
        unsigned raw[3];
        auto ask_params = [&]() {
            ask_tag();
            const unsigned st = (unsigned)(128 * ct + (tid & 127)) * 2u;
            raw[0] = gload_u16(st, p.logdt); raw[1] = gload_u16(st, p.lre); raw[2] = gload_u16(st, p.lim);
        };
        f32x4 acc[5][4];
        auto params = [&]() {
            take_tag();
            asm volatile("; TIE %0 %1 %2" : "+v"(raw[0]), "+v"(raw[1]), "+v"(raw[2]));
            if (w < 2) {
                float a, bq, cc, dd, xr, xi, yr, yi;
                ssm_params(raw[0], raw[1], raw[2], a, bq, cc, dd);
                *(LAS f32x4*)(size_t)(lds0 + LDS_TAB + tid * 16) = (f32x4){a, bq, cc, dd};
                pow20(a, bq, t20r, t20i);
                cmul(t20r, t20i, t20r, t20i, xr, xi); cmul(xr, xi, xr, xi, yr, yi); cmul(yr, yi, yr, yi, t160r, t160i);
                WAITL0();
            }
        };
        if (w < 4) p1_main<true, 0>(p, acc, pan, ct, w, lane, lds0, ask_params, setup, params, warm, []() {}, []() {});
        else p1_main<true, 1>(p, acc, pan, ct, w, lane, lds0, ask_tag, setup, take_tag, warm, []() {}, []() {});
        float lr[2], li[2], cr[2], ci[2];
#pragma unroll
        for (int n = 0; n < 2; ++n) {
            const f32x4 t = *(const LAS f32x4*)(size_t)(lds0 + LDS_TAB + (32 * c + 16 * n + sg) * 16);
            lr[n] = t[0]; li[n] = t[1]; cr[n] = t[2]; ci[n] = t[3];
        }
        const int seg = 4 * th + q;
        const unsigned ea = lds0 + LDS_E + (seg * 128 + 32 * c + sg) * 8;
        const float nli[2] = {-li[0], -li[1]}, nci[2] = {-ci[0], -ci[1]};
        {
            float er0 = 0.f, ei0 = 0.f, er1 = 0.f, ei1 = 0.f;
#pragma unroll
            for (int i = 0; i < 20; i += 2)
                horner2(er0, ei0, er1, ei1, lr[0], li[0], nli[0], lr[1], li[1], nli[1],
                        acc[i >> 2][0][i & 3], acc[i >> 2][2][i & 3], acc[i >> 2][1][i & 3], acc[i >> 2][3][i & 3],
                        acc[i >> 2][0][(i & 3) + 1], acc[i >> 2][2][(i & 3) + 1], acc[i >> 2][1][(i & 3) + 1], acc[i >> 2][3][(i & 3) + 1]);
            *(LAS f32x2*)(size_t)ea = (f32x2){cr[0] * er0 - ci[0] * ei0, cr[0] * ei0 + ci[0] * er0};
            *(LAS f32x2*)(size_t)(ea + 128) = (f32x2){cr[1] * er1 - ci[1] * ei1, cr[1] * ei1 + ci[1] * er1};
        }
        WAITL0(); BAR();
        float pr[8], pi[8];
        if (w < 2) {
            float tr = 0.f, tii = 0.f;
#pragma unroll
            for (int j = 0; j < 8; ++j) {
                const f32x2 e = *(const LAS f32x2*)(size_t)(lds0 + LDS_E + (j * 128 + tid) * 8);
                pr[j] = tr; pi[j] = tii;
                const float t = t20r * tr - t20i * tii + e[0];
                tii = t20r * tii + t20i * tr + e[1]; tr = t;
            }
            if (pan < NPAN - 1) {
                const u32x4 gv = {__float_as_uint(tr), tag0, __float_as_uint(tii), tag1};
                const __amdgpu_buffer_rsrc_t gr = __builtin_amdgcn_make_buffer_rsrc(p.ws + WS_GRAN, 0, 12 * 640 * 16, 0x00020000);
                __builtin_amdgcn_raw_buffer_store_b128(gv, gr, (unsigned)(pan * 640 + 128 * ct + tid) * 16u, 0, 16);
            }
        }
#pragma unroll
        for (int k = 0; k < 10; ++k) {
            const int n = k & 1, bb = k >> 1;
            cscale4(acc[bb][n], acc[bb][2 + n], cr[n], ci[n], nci[n]);
            if (k < 9) w2_piece<false>(p, src, k / 3, tj, w, k % 3);
            SCHED();
        }
        if (w < 2) {
            float hr = 0.f, hi = 0.f;
            if (pan > 0) {
                const __amdgpu_buffer_rsrc_t gr = __builtin_amdgcn_make_buffer_rsrc(p.ws + WS_GRAN, 0, 12 * 640 * 16, 0x00020000);
                u32x4 gv[12];
                for (unsigned spins = 0;; ++spins) {
                    asm volatile("" ::: "memory");
                    bool ok = true;
#pragma unroll
                    for (int j = 0; j < 12; ++j) {
                        const int jj = j < pan ? j : pan - 1;
                        gv[j] = __builtin_amdgcn_raw_buffer_load_b128(gr, (unsigned)(jj * 640 + 128 * ct + tid) * 16u, 0, 16);
                    }
#pragma unroll
                    for (int j = 0; j < 12; ++j) ok &= (gv[j][1] == tag0) & (gv[j][3] == tag1);
                    if (__all(ok) || spins > SPIN_CAP) break;
                    __builtin_amdgcn_s_sleep(1);
                }
#pragma unroll
                for (int j = 0; j < 12; ++j) {
                    if (j < pan) {
                        const float t = t160r * hr - t160i * hi + __uint_as_float(gv[j][0]);
                        hi = t160r * hi + t160i * hr + __uint_as_float(gv[j][2]); hr = t;
                    }
                }
            }
#pragma unroll
            for (int j = 0; j < 8; ++j) {
                *(LAS f32x2*)(size_t)(lds0 + LDS_E + (j * 128 + tid) * 8) = (f32x2){pr[j] + hr, pi[j] + hi};
                const float t = t20r * hr - t20i * hi;
                hi = t20r * hi + t20i * hr; hr = t;
            }
        }
        WAITL0(); BAR();
        const unsigned trb = lds0 + LDS_TR + w * TR_WAVE;
        {
            f32x2 cw[2];
#pragma unroll
            for (int n = 0; n < 2; ++n) cw[n] = *(const LAS f32x2*)(size_t)(ea + n * 128);
            float hr[2] = {cw[0][0], cw[1][0]}, hi[2] = {cw[0][1], cw[1][1]};
            const unsigned ta = trb + sg * TR_STRIDE + 40 * q;
#pragma unroll
            for (int bb = 0; bb < 5; ++bb) {
#pragma unroll
                for (int n = 0; n < 2; ++n) {
                    u32x2 ore, oim;
                    scan4(hr[n], hi[n], acc[bb][n], acc[bb][2 + n], lr[n], li[n], nli[n], ore, oim);
                    const unsigned a = ta + 16 * n * TR_STRIDE + 8 * bb;
                    *(LAS u32x2*)(size_t)a = ore;
                    *(LAS u32x2*)(size_t)(a + 32 * TR_STRIDE) = oim;
                }
            }
        }
        WAITL0();
        {
            const int qq = (lane & 15) >> 2, pp = lane & 3;
            const unsigned rb = trb + (8 * q + qq) * TR_STRIDE + 8 * pp;
#pragma unroll
            for (int comp = 0; comp < 2; ++comp)
#pragma unroll
                for (int m = 0; m < 5; ++m) {
                    const unsigned a = rb + comp * 32 * TR_STRIDE + m * 32;
                    const bf16x8 v = ldstr(a, a + 4 * TR_STRIDE);
                    const int tb = 10 * pan + 5 * th + m, s2 = KSF + comp * 20 + 4 * ct + c;
                    if (tb < 128) __builtin_amdgcn_raw_buffer_store_b128(__builtin_bit_cast(u32x4, v), a2r, (unsigned)(tb * KS2 + s2) * 1024u + lane * 16, 0, 16);
                }
        }
        WAITV(0); WAITL0(); BAR();
        if (w == 0) __hip_atomic_store((GAS unsigned long long*)(p.ws + flag_off), tag, RLX);
#pragma unroll
        for (int k = 9; k < 3 * (int)NSLOT; ++k) w2_piece<false>(p, src, k / 3, tj, w, k % 3);
    } else {
        const int u0 = 30 * pan;
        ask_tag();
#pragma unroll
        for (int k = 0; k < 15; ++k) wt_request(p, u0 + k, w, lane, lds0 + k * 8192);
        WAITV(0); take_tag();
        BAR();
#pragma unroll
        for (int k = 0; k < 15; ++k) wt_store(p, u0 + k, w, lane, wt_read(w, lane, lds0 + k * 8192));
        WAITL0(); BAR();
#pragma unroll
        for (int k = 0; k < 15; ++k) wt_request(p, u0 + 15 + k, w, lane, lds0 + k * 8192);
        setup();
        WAITV(0); BAR();
#pragma unroll
        for (int k = 0; k < 15; ++k) wt_store(p, u0 + 15 + k, w, lane, wt_read(w, lane, lds0 + k * 8192));
        WAITL0();
        WAITV(0); BAR();
        if (w == 0) {
            __hip_atomic_store((GAS unsigned long long*)(p.ws + wt_flag(u0 + min(lane, 29))), tag, RLX);
            __hip_atomic_store((GAS unsigned long long*)(p.ws + flag_off), tag, RLX);
        }
        if (w >= 4) warm();
        if (down) {
            spin_flags(p.ws, tf0, tag); spin_flags(p.ws, tf1, tag);
#pragma unroll
            for (int k = 0; k < 3 * (int)NSLOT; ++k) w2_piece<true>(p, src, k / 3, tj, w, k % 3);
        } else {
#pragma unroll
            for (int k = 0; k < 3 * (int)NSLOT; ++k) w2_piece<false>(p, src, k / 3, tj, w, k % 3);
        }
    }
    if (w == 0) {
        const int p0 = (128 * ti) / PM, p1 = (128 * ti + 127) / PM, k = lane & 15;
        spin_flags(p.ws, WS_FLAG + (((lane >> 4) & 1 ? p1 : p0) * 32 + (down ? 0 : 16) + (k < 5 ? k : 0)) * 8, tag);
    }
    WAITV(0);
    BAR();
    const int tb = 8 * ti + 2 * (w & 3) + (w >> 2), t = 16 * tb + (lane & 15), q = lane >> 4;
    u32x4 gq;
    f32x4 acc2[2][5];
    const unsigned voff = (unsigned)(pair * 8 + (tb & 7)) * 3072u + lane * 16, vflag = WS_VFLAG + pair * 64;
    const __amdgpu_buffer_rsrc_t vr = __builtin_amdgcn_make_buffer_rsrc(p.ws + WS_V, 0, V_BYTES, 0x00020000);
    const int d0 = 80 * tj + 4 * q;
    auto gate = [&]() {
        if (!__all(gq[1] == tag0 && gq[3] == tag1)) {
            const __amdgpu_buffer_rsrc_t gr = __builtin_amdgcn_make_buffer_rsrc(p.ws + WS_GATE, 0, 2048 * 16, 0x00020000);
            for (unsigned spins = 0;; ++spins) {
                asm volatile("" ::: "memory");
                gq = __builtin_amdgcn_raw_buffer_load_b128(gr, (unsigned)t * 16u, 0, 16);
                if (__all(gq[1] == tag0 && gq[3] == tag1) || spins > SPIN_CAP) break;
                __builtin_amdgcn_s_sleep(1);
            }
        }
        return __uint_as_float(gq[0]);
    };
    if (down) {
        p2_loop<true, 1>(p, acc2, src, ti, tj, w, lane, lds0, [&]() { gq = gload_sc1((unsigned)t * 16u, p.ws + WS_GATE); },
                         [&]() { asm volatile("; TIE %0" : "+v"(gq)); }, tag);
        p2_join(acc2, w, lane, lds0);
        const float omg = rbf(1.0f - gate());
        unsigned pk[12];
#pragma unroll
        for (int n = 0; n < 5; ++n) {
            pk[2 * n] = pack2(omg * rbf(acc2[0][n][0]), omg * rbf(acc2[0][n][1]));
            pk[2 * n + 1] = pack2(omg * rbf(acc2[0][n][2]), omg * rbf(acc2[0][n][3]));
        }
        pk[10] = pk[11] = 0u;
#pragma unroll
        for (int k = 0; k < 3; ++k) __builtin_amdgcn_raw_buffer_store_b128((u32x4){pk[4 * k], pk[4 * k + 1], pk[4 * k + 2], pk[4 * k + 3]}, vr, voff + k * 1024, 0, 16);
        WAITV(0); BAR();
        if (w == 0) __hip_atomic_store((GAS unsigned long long*)(p.ws + vflag), tag, RLX);
    } else {
        u32x2 xv[5], dv[5], vf;
        p2_loop<false, 7>(p, acc2, src, ti, tj, w, lane, lds0, [&]() {
            gq = gload_sc1((unsigned)t * 16u, p.ws + WS_GATE);
            const unsigned dof = (unsigned)d0 * 2u;
            const u16* dsk = late_args()->dsk;
#pragma unroll
            for (int n = 0; n < 5; ++n) asm volatile("global_load_dwordx2 %0, %1, %2 offset:%3" : "=v"(dv[n]) : "v"(dof), "s"(dsk), "n"(32 * n) : "memory");
            asm volatile("global_load_dwordx2 %0, %1, %2 sc1" : "=v"(vf) : "v"(vflag), "s"(p.ws) : "memory");
        }, []() {}, tag);
        WAITV(0);
        asm volatile("; TIE %0 %1 %2 %3 %4 %5 %6" : "+v"(dv[0]), "+v"(dv[1]), "+v"(dv[2]), "+v"(dv[3]), "+v"(dv[4]), "+v"(gq), "+v"(vf));
        const bool early = __all(vf[0] == tag0 && vf[1] == tag1);
        u32x4 vv[3];
        if (early) {
#pragma unroll
            for (int k = 0; k < 3; ++k) vv[k] = __builtin_amdgcn_raw_buffer_load_b128(vr, voff + k * 1024, 0, 16);
        }
#pragma unroll
        for (int n = 0; n < 5; ++n) xv[n] = *(const LAS u32x2*)(size_t)(lds0 + 3 * S2 + (t - 128 * ti) * 160 + 32 * n + 8 * q);
        p2_join(acc2, w, lane, lds0);
        const float g = gate();
        f32x4 u4[5];
#pragma unroll
        for (int n = 0; n < 5; ++n)
#pragma unroll
            for (int e = 0; e < 4; ++e) {
                const unsigned xw = xv[n][e >> 1], dw = dv[n][e >> 1];
                u4[n][e] = g * (acc2[0][n][e] + rbf(((e & 1) ? bfhi(xw) : bflo(xw)) * ((e & 1) ? bfhi(dw) : bflo(dw))));
            }
        if (w == 0 && !early) spin_flags(p.ws, vflag, tag);
        BAR();
        if (!early) {
#pragma unroll
            for (int k = 0; k < 3; ++k) vv[k] = __builtin_amdgcn_raw_buffer_load_b128(vr, voff + k * 1024, 0, 16);
        }
        const unsigned ob = lds0 + 4 * S2 + w * 5376;
#pragma unroll
        for (int n = 0; n < 5; ++n) {
            f32x4 o4;
#pragma unroll
            for (int e = 0; e < 4; ++e) {
                const unsigned vw = vv[(2 * n + (e >> 1)) >> 2][(2 * n + (e >> 1)) & 3];
                o4[e] = u4[n][e] + ((e & 1) ? bfhi(vw) : bflo(vw));
            }
            *(LAS f32x4*)(size_t)(ob + (lane & 15) * 336 + 64 * n + 16 * q) = o4;
        }
        WAITL0();
        const __amdgpu_buffer_rsrc_t orr = __builtin_amdgcn_make_buffer_rsrc(late_args()->out, 0, L * D * 4, 0x00020000);
#pragma unroll
        for (int i = 0; i < 5; ++i) {
            const int c = 64 * i + lane, row = c / 20, col = c - 20 * row;
            const u32x4 v = *(const LAS u32x4*)(size_t)(ob + row * 336 + col * 16);
            __builtin_amdgcn_raw_buffer_store_b128(v, orr, (unsigned)((16 * tb + row) * D + 80 * tj) * 4u + col * 16, 0, 16);
        }
    }
    if (b == 0 && w == 7) {
        for (unsigned spins = 0;; ++spins) {
            bool ok = true;
#pragma unroll
            for (int k = 0; k < 7; ++k) {
                const int idx = 64 * k + lane, pn = idx >> 5, sl = idx & 31;
                const bool used = pn < 13 ? (sl < 14 || (sl >= 16 && sl < 21)) : sl < 9;
                ok &= ld_flag(p.ws, WS_FLAG + (used ? idx : 0) * 8) == tag;
            }
            if (__all(ok) || spins > SPIN_CAP) break;
            __builtin_amdgcn_s_sleep(1);
        }
        if (lane == 0) __hip_atomic_store((GAS unsigned long long*)(p.ws + WS_EPOCH), tag, RLX);
    }
    asm volatile(".Lmix_code_end:");
}

}

extern "C" void kernel_launch(void* const* d_in, const int* in_sizes, int n_in, void* d_out, int out_size, void* d_ws, size_t ws_size,
                              hipStream_t stream) {
    Params p;
    p.x = (const u16*)d_in[0]; p.lre = (const u16*)d_in[1]; p.lim = (const u16*)d_in[2];
    p.bre = (const u16*)d_in[3]; p.bim = (const u16*)d_in[4]; p.cre = (const u16*)d_in[5]; p.cim = (const u16*)d_in[6];
    p.logdt = (const u16*)d_in[7]; p.dsk = (const u16*)d_in[8];
    p.wg = (const u16*)d_in[10]; p.wu = (const u16*)d_in[11]; p.wd = (const u16*)d_in[12]; p.gw = (const u16*)d_in[13];
    p.out = (float*)d_out; p.ws = (char*)d_ws;
    hipLaunchKernelGGL(s5_swiglu_mix_kernel, dim3(256), dim3(512), 0, stream, p);
}
```
